```python
import torch

def _softmax(x, axis=-1):
    return torch.softmax(x, dim=axis)
def _gelu(x):
    return torch.nn.functional.gelu(x, approximate="tanh")
def _silu(x):
    return torch.nn.functional.silu(x)
def _sigmoid(x):
    return torch.sigmoid(x)
def _layernorm(x, axis=-1, eps=1e-5):
    m = x.mean(dim=axis, keepdim=True)
    v = x.var(dim=axis, keepdim=True, unbiased=False)
    return (x - m) / torch.sqrt(v + eps)
def _scatter_add(tgt, idx, src, axis=0):
    return tgt.clone().index_add_(axis, idx, src)
def _segment_sum(src, seg, n):
    out = torch.zeros((n,) + src.shape[1:], dtype=src.dtype)
    return out.index_add_(0, seg, src)
def _pad(x, pw):
    flat = tuple(v for pair in reversed(pw) for v in pair)
    return torch.nn.functional.pad(x, flat)

kernel_name = 'parallel_sum__gqa_block_cached__dense_activation_silu__decode'

def setup_inputs():

    _w = (lambda a: (lambda t: t.to(torch.bfloat16) if t.is_floating_point() else t)(torch.as_tensor(a)))
    def _A_setup():
        B, Sq, D = 2, 1, 1024
        Hq, Hkv, Sk, Dh = 32, 4, 32768, 128
        assert Hq % Hkv == 0, (Hq, Hkv)
        return {
            "x": _w((lambda *s: torch.randn(*s, dtype=torch.bfloat16) * 0.05)(B, Sq, D)),
            "k_cache": _w((lambda *s: torch.randn(*s, dtype=torch.bfloat16) * 3.2)(B, Hkv, Sk, Dh)),
            "v_cache": _w((lambda *s: torch.randn(*s, dtype=torch.bfloat16) * 0.8)(B, Hkv, Sk, Dh)),
            "wq": _w((lambda *s: torch.randn(*s, dtype=torch.bfloat16) * 3.2)(D, Hq * Dh) * (D ** -0.5)),
            "wo": _w((lambda *s: torch.randn(*s, dtype=torch.bfloat16) * 0.8)(Hq * Dh, D) * ((Hq * Dh) ** -0.5)),
        }
    _A_in = _A_setup()

    def _B_setup():
        B, S, D, F = 2, 1, 1024, 3584
        return {
            "x": _w((lambda *s: torch.randn(*s, dtype=torch.bfloat16) * 0.05)(B, S, D)),
            "w1": _w((lambda *s: torch.randn(*s, dtype=torch.bfloat16) * 3.2)(D, F) * (D ** -0.5)),
            "w2": _w((lambda *s: torch.randn(*s, dtype=torch.bfloat16) * 0.4)(F, D) * (F ** -0.5)),
        }
    _B_in = _B_setup()

    return {
        "A_x": _A_in["x"],
        "A_k_cache": _A_in["k_cache"],
        "A_v_cache": _A_in["v_cache"],
        "A_wq": _A_in["wq"],
        "A_wo": _A_in["wo"],
        "B_x": _B_in["x"],
        "B_w1": _B_in["w1"],
        "B_w2": _B_in["w2"],
    }

def reference(A_x, A_k_cache, A_v_cache, A_wq, A_wo, B_x, B_w1, B_w2):
    def _A_ref(x, k_cache, v_cache, wq, wo):
        B, Sq, D = x.shape
        Hq, Dh = 32, 128
        Hkv = k_cache.shape[1]
        rep = Hq // Hkv
        q = (lambda x, *d: x.permute(*d))((x @ wq).reshape(B, Sq, Hq, Dh), 0, 2, 1, 3)
        k = (lambda x, n, axis: torch.repeat_interleave(x, n, dim=axis))(k_cache, rep, axis=1)
        v = (lambda x, n, axis: torch.repeat_interleave(x, n, dim=axis))(v_cache, rep, axis=1)
        scores = torch.einsum("bhqd,bhkd->bhqk", q, k) * (Dh ** -0.5)
        p = _softmax(scores, axis=-1)
        o = torch.einsum("bhqk,bhkd->bhqd", p, v)
        return (lambda x, *d: x.permute(*d))(o, 0, 2, 1, 3).reshape(B, Sq, Hq * Dh) @ wo

    def _B_ref(x, w1, w2):
        h = torch.einsum("bsd,df->bsf", x, w1)
        h = _silu(h)
        out = torch.einsum("bsf,fd->bsd", h, w2)
        return out

    x = A_x
    xn = _layernorm(x, axis=-1)
    return x + _A_ref(xn, A_k_cache, A_v_cache, A_wq, A_wo) + _B_ref(xn, B_w1, B_w2)
```

```cpp
#include <hip/hip_runtime.h>
#include <hip/hip_bf16.h>
#include <stdint.h>

#define MIMIC_SCORE 0

typedef unsigned v4u __attribute__((ext_vector_type(4)));
typedef float f32x4 __attribute__((ext_vector_type(4)));
typedef short bf16x8 __attribute__((ext_vector_type(8)));
typedef unsigned short u16;

constexpr int D = 1024, HQ = 32, HKV = 4, SK = 32768, DH = 128, F = 3584;
constexpr int NSPLIT = 32;

__device__ __forceinline__ float bf2f(unsigned b) { return __builtin_bit_cast(float, b << 16); }
typedef float f32x2 __attribute__((ext_vector_type(2)));
typedef __bf16 bf16x2 __attribute__((ext_vector_type(2)));
__device__ __forceinline__ unsigned cvtpk(float lo, float hi) {
  return __builtin_bit_cast(unsigned, __builtin_convertvector(f32x2{lo, hi}, bf16x2));
}
__device__ __forceinline__ float rbf(float x) { return __builtin_bit_cast(float, cvtpk(x, x) & 0xffff0000u); }
__device__ __forceinline__ f32x4 mfma(v4u a, v4u b, f32x4 c) {
  return __builtin_amdgcn_mfma_f32_16x16x32_bf16(__builtin_bit_cast(bf16x8, a), __builtin_bit_cast(bf16x8, b), c, 0, 0, 0);
}
__device__ __forceinline__ float xrow16_max(float x) {
  auto s = __builtin_amdgcn_permlane16_swap(__float_as_uint(x), __float_as_uint(x), false, false);
  x = fmaxf(__uint_as_float(s[0]), __uint_as_float(s[1]));
  auto t = __builtin_amdgcn_permlane32_swap(__float_as_uint(x), __float_as_uint(x), false, false);
  return fmaxf(__uint_as_float(t[0]), __uint_as_float(t[1]));
}
__device__ __forceinline__ float xrow16_sum(float x) {
  auto s = __builtin_amdgcn_permlane16_swap(__float_as_uint(x), __float_as_uint(x), false, false);
  x = __uint_as_float(s[0]) + __uint_as_float(s[1]);
  auto t = __builtin_amdgcn_permlane32_swap(__float_as_uint(x), __float_as_uint(x), false, false);
  return __uint_as_float(t[0]) + __uint_as_float(t[1]);
}

__device__ void layernorm_lds(const u16* x, float* xn  , float* red  , int tid, int nthr) {
  const int wave = tid >> 6, lane = tid & 63;
  if (wave < 2) {
    const u16* xr = x + wave * D;
    float v[16]; float s = 0.f;
    for (int i = 0; i < 16; ++i) { v[i] = bf2f(xr[lane * 16 + i]); s += v[i]; }
    for (int m = 32; m >= 1; m >>= 1) s += __shfl_xor(s, m);
    const float mean = s * (1.f / D);
    float q = 0.f;
    for (int i = 0; i < 16; ++i) { const float d = v[i] - mean; q += d * d; }
    for (int m = 32; m >= 1; m >>= 1) q += __shfl_xor(q, m);
    const float var = rbf(q * (1.f / D));
    const float mb = rbf(mean);
    const float den = rbf(sqrtf(rbf(var + 1e-5f)));
    for (int i = 0; i < 16; ++i) xn[wave * D + lane * 16 + i] = rbf(rbf(v[i] - mb) / den);
  }
  __syncthreads();
}

__global__ void __launch_bounds__(256) k1(const u16* x, const u16* wq, const u16* w1, u16* q, u16* h) {
  __shared__ float xn[2 * D];
  __shared__ float red[8][2][64];
  const int tid = threadIdx.x;
  layernorm_lds(x, xn, nullptr, tid, 256);
  const int blk = blockIdx.x;
  const bool isq = blk < 64;
  const u16* w = isq ? wq : w1;
  const int N = isq ? 4096 : F;
  const int col0 = (isq ? blk : blk - 64) * 64;
  const int c8 = tid & 7, rg = tid >> 3;
  float a0[8], a1[8];
  for (int j = 0; j < 8; ++j) { a0[j] = 0.f; a1[j] = 0.f; }
  for (int r = rg; r < D; r += 32) {
    const v4u wv = *(const v4u*)(w + (size_t)r * N + col0 + c8 * 8);
    const float x0 = xn[r], x1 = xn[D + r];
    for (int k = 0; k < 4; ++k) {
      const float lo = bf2f(wv[k] & 0xffffu), hi = bf2f(wv[k] >> 16);
      a0[2 * k] += x0 * lo; a0[2 * k + 1] += x0 * hi;
      a1[2 * k] += x1 * lo; a1[2 * k + 1] += x1 * hi;
    }
  }
  for (int j = 0; j < 8; ++j) for (int m = 8; m <= 32; m <<= 1) { a0[j] += __shfl_xor(a0[j], m); a1[j] += __shfl_xor(a1[j], m); }
  const int wave = tid >> 6, lane = tid & 63;
  if (lane < 8) for (int j = 0; j < 8; ++j) { red[wave][0][lane * 8 + j] = a0[j]; red[wave][1][lane * 8 + j] = a1[j]; }
  __syncthreads();
  if (tid < 128) {
    const int b = tid >> 6, c = tid & 63;
    float s = red[0][b][c] + red[1][b][c] + red[2][b][c] + red[3][b][c];
    s = rbf(s);
    if (isq) q[b * 4096 + col0 + c] = (u16)(__builtin_bit_cast(unsigned, s) >> 16);
    else {
      const float si = rbf(s / (1.f + __expf(-s)));
      h[b * F + col0 + c] = (u16)(__builtin_bit_cast(unsigned, si) >> 16);
    }
  }
}

__global__ void __launch_bounds__(512, 2) k2(const u16* __restrict__ q, const u16* __restrict__ kc, const u16* __restrict__ vc,
                                             float* __restrict__ po, float* __restrict__ pml) {
  __shared__ __attribute__((aligned(16))) float lds[8 * 1024 + 8 * 16];
  const int tid = threadIdx.x, lane = tid & 63, w = __builtin_amdgcn_readfirstlane(tid >> 6);
  const int pair = blockIdx.x & 7, split = blockIdx.x >> 3;
  const int b = pair >> 2, kvh = pair & 3;
  const size_t kvbase = ((size_t)pair * SK + split * 1024 + w * 128) * DH;
  const int g = lane >> 4, n = lane & 15;
  const int headn = 2 * (n & 3) + (n >> 3), bpp = (n >> 2) & 1;
  v4u qb[2];
#pragma unroll
  for (int t = 0; t < 2; ++t) qb[t] = *(const v4u*)(q + b * 4096 + (kvh * 8 + headn) * DH + 8 * (4 * g + 2 * t + bpp));
  const int ka = (lane >> 2) & 3, kb4 = lane & 3;
  const unsigned char* kp = (const unsigned char*)(kc + kvbase) + ka * 256 + (4 * g + kb4) * 16;
  const unsigned char* vp = (const unsigned char*)(vc + kvbase) + lane * 16;
  float S[32];
#pragma unroll
  for (int u = 0; u < 32; ++u) {
    const v4u kv = __builtin_nontemporal_load((const v4u*)(kp + u * 1024));
    const f32x4 z = {0.f, 0.f, 0.f, 0.f};
    const f32x4 d0 = mfma(kv, qb[0], z), d1 = mfma(kv, qb[1], z);
    const float t1 = d0[0] + d1[2], t2 = d0[1] + d1[3];
    const float own = bpp ? t2 : t1;
    const float oth = __shfl_xor(own, 4);
    S[u] = own + oth;
  }
  float mx = S[0];
#pragma unroll
  for (int u = 1; u < 32; ++u) mx = fmaxf(mx, S[u]);
  mx = xrow16_max(mx);
  constexpr float SCALE = 0.08838834764831845f, LOG2E = 1.4426950408889634f;
  const float mr = mx * SCALE;
  const float mc = mr * LOG2E;
  float lsum = 0.f;
  unsigned P[32];
#pragma unroll
  for (int u = 0; u < 32; ++u) {
    const float sr = S[u] * SCALE;
    const float p = __builtin_amdgcn_exp2f(sr * LOG2E - mc);
    lsum += p;
    P[u] = cvtpk(p, p);
  }
  lsum = xrow16_sum(lsum);
  unsigned mask[4];
  {
    const int jp = lane & 7;
#pragma unroll
    for (int k = 0; k < 4; ++k) mask[k] = ((jp >> 1) == k) ? ((jp & 1) ? 0xffff0000u : 0x0000ffffu) : 0u;
  }
  f32x4 acc[4];
#pragma unroll
  for (int t = 0; t < 4; ++t) acc[t] = f32x4{0.f, 0.f, 0.f, 0.f};
#pragma unroll
  for (int u = 0; u < 32; ++u) {
    const v4u vv = __builtin_nontemporal_load((const v4u*)(vp + u * 1024));
    const unsigned p0 = __builtin_amdgcn_update_dpp(0, P[u], 0x00, 0xf, 0xf, true);
    const unsigned p1 = __builtin_amdgcn_update_dpp(0, P[u], 0x55, 0xf, 0xf, true);
    const unsigned p2 = __builtin_amdgcn_update_dpp(0, P[u], 0xAA, 0xf, 0xf, true);
    const unsigned p3 = __builtin_amdgcn_update_dpp(0, P[u], 0xFF, 0xf, 0xf, true);
    acc[0] = mfma(v4u{p0 & mask[0], p0 & mask[1], p0 & mask[2], p0 & mask[3]}, vv, acc[0]);
    acc[1] = mfma(v4u{p1 & mask[0], p1 & mask[1], p1 & mask[2], p1 & mask[3]}, vv, acc[1]);
    acc[2] = mfma(v4u{p2 & mask[0], p2 & mask[1], p2 & mask[2], p2 & mask[3]}, vv, acc[2]);
    acc[3] = mfma(v4u{p3 & mask[0], p3 & mask[1], p3 & mask[2], p3 & mask[3]}, vv, acc[3]);
  }
#pragma unroll
  for (int t = 0; t < 4; ++t) *(f32x4*)(lds + w * 1024 + t * 256 + lane * 4) = acc[t];
  float* ml = lds + 8 * 1024;
  if (lane < 16 && bpp == 0) { ml[w * 16 + headn * 2] = mr; ml[w * 16 + headn * 2 + 1] = lsum; }
  __syncthreads();
  if (tid < 256) {
    const int t = tid >> 6, ap = lane >> 4, c = lane & 15;
    const int head = 2 * t + (ap >> 1);
    float M = ml[head * 2];
#pragma unroll
    for (int ww = 1; ww < 8; ++ww) M = fmaxf(M, ml[ww * 16 + head * 2]);
    f32x4 o = {0.f, 0.f, 0.f, 0.f}; float L = 0.f;
#pragma unroll
    for (int ww = 0; ww < 8; ++ww) {
      const float wt = __builtin_amdgcn_exp2f((ml[ww * 16 + head * 2] - M) * LOG2E);
      L += wt * ml[ww * 16 + head * 2 + 1];
      const f32x4 ov = *(const f32x4*)(lds + ww * 1024 + t * 256 + lane * 4);
      o += wt * ov;
    }
    const size_t row = ((size_t)(b * HQ + kvh * 8 + head) * NSPLIT + split);
    *(f32x4*)(po + row * DH + 8 * c + 4 * (ap & 1)) = o;
    if (c == 0 && (ap & 1) == 0) { pml[row * 2] = M; pml[row * 2 + 1] = L; }
  }
}

__global__ void __launch_bounds__(128) k3a(const float* po, const float* pml, u16* o) {
  const int bh = blockIdx.x, d = threadIdx.x;
  float M = -INFINITY;
  for (int s = 0; s < NSPLIT; ++s) M = fmaxf(M, pml[(bh * NSPLIT + s) * 2]);
  float L = 0.f, a = 0.f;
  for (int s = 0; s < NSPLIT; ++s) {
    const float wt = __builtin_amdgcn_exp2f((pml[(bh * NSPLIT + s) * 2] - M) * 1.4426950408889634f);
    L += wt * pml[(bh * NSPLIT + s) * 2 + 1];
    a += wt * po[(size_t)(bh * NSPLIT + s) * DH + d];
  }
  const float r = rbf(a / L);
  o[bh * DH + d] = (u16)(__builtin_bit_cast(unsigned, r) >> 16);
}

__global__ void __launch_bounds__(256) k3b(const u16* x, const u16* o, const u16* h, const u16* wo, const u16* w2, u16* out) {
  __shared__ float red[4][2][2][64];
  const int tid = threadIdx.x, col0 = blockIdx.x * 64;
  const int c8 = tid & 7, rg = tid >> 3;
  float a[2][2][8];
  for (int p = 0; p < 2; ++p) for (int b = 0; b < 2; ++b) for (int j = 0; j < 8; ++j) a[p][b][j] = 0.f;
  for (int r = rg; r < 4096; r += 32) {
    const v4u wv = *(const v4u*)(wo + (size_t)r * D + col0 + c8 * 8);
    const float x0 = bf2f(o[r]), x1 = bf2f(o[4096 + r]);
    for (int k = 0; k < 4; ++k) {
      const float lo = bf2f(wv[k] & 0xffffu), hi = bf2f(wv[k] >> 16);
      a[0][0][2 * k] += x0 * lo; a[0][0][2 * k + 1] += x0 * hi;
      a[0][1][2 * k] += x1 * lo; a[0][1][2 * k + 1] += x1 * hi;
    }
  }
  for (int r = rg; r < F; r += 32) {
    const v4u wv = *(const v4u*)(w2 + (size_t)r * D + col0 + c8 * 8);
    const float x0 = bf2f(h[r]), x1 = bf2f(h[F + r]);
    for (int k = 0; k < 4; ++k) {
      const float lo = bf2f(wv[k] & 0xffffu), hi = bf2f(wv[k] >> 16);
      a[1][0][2 * k] += x0 * lo; a[1][0][2 * k + 1] += x0 * hi;
      a[1][1][2 * k] += x1 * lo; a[1][1][2 * k + 1] += x1 * hi;
    }
  }
  for (int p = 0; p < 2; ++p) for (int b = 0; b < 2; ++b) for (int j = 0; j < 8; ++j)
    for (int m = 8; m <= 32; m <<= 1) a[p][b][j] += __shfl_xor(a[p][b][j], m);
  const int wave = tid >> 6, lane = tid & 63;
  if (lane < 8) for (int p = 0; p < 2; ++p) for (int b = 0; b < 2; ++b) for (int j = 0; j < 8; ++j) red[wave][p][b][lane * 8 + j] = a[p][b][j];
  __syncthreads();
  if (tid < 128) {
    const int b = tid >> 6, c = tid & 63;
    const float at = rbf(red[0][0][b][c] + red[1][0][b][c] + red[2][0][b][c] + red[3][0][b][c]);
    const float ff = rbf(red[0][1][b][c] + red[1][1][b][c] + red[2][1][b][c] + red[3][1][b][c]);
    const float xv = bf2f(x[b * D + col0 + c]);
    const float r = rbf(rbf(xv + at) + ff);
    out[b * D + col0 + c] = (u16)(__builtin_bit_cast(unsigned, r) >> 16);
  }
}

extern "C" void kernel_launch(void* const* d_in, const int* in_sizes, int n_in, void* d_out, int out_size, void* d_ws, size_t ws_size,
                              hipStream_t stream) {
  const u16* x = (const u16*)d_in[0];
  const u16* kc = (const u16*)d_in[1];
  const u16* vc = (const u16*)d_in[2];
  const u16* wq = (const u16*)d_in[3];
  const u16* wo = (const u16*)d_in[4];
  const u16* w1 = (const u16*)d_in[6];
  const u16* w2 = (const u16*)d_in[7];
  unsigned char* ws = (unsigned char*)d_ws;
  u16* q = (u16*)ws;
  u16* h = (u16*)(ws + 16384);
  u16* o = (u16*)(ws + 32768);
  float* pml = (float*)(ws + 65536);
  float* po = (float*)(ws + 131072);
  k1<<<64 + 56, 256, 0, stream>>>(x, wq, w1, q, h);
  k2<<<256, 512, 0, stream>>>(q, kc, vc, po, pml);
  k3a<<<64, 128, 0, stream>>>(po, pml, o);
  k3b<<<16, 256, 0, stream>>>(x, o, h, wo, w2, (u16*)d_out);
}
```

```cpp
#include <hip/hip_runtime.h>
#include <stdint.h>

typedef unsigned v4u __attribute__((ext_vector_type(4)));
typedef unsigned v2u __attribute__((ext_vector_type(2)));
typedef float f32x4 __attribute__((ext_vector_type(4)));
typedef float f32x2 __attribute__((ext_vector_type(2)));
typedef short bf16x8 __attribute__((ext_vector_type(8)));
typedef __bf16 bf16x2 __attribute__((ext_vector_type(2)));
typedef unsigned short u16;

#define PAT_C 4
#define PAT_STG 4
#define PAT_ROT 1

#define BIAS 0
#define EARLY_K 8
#define GWAIT 3
#define POLL_SLEEP 2
#define FIN_DELAY 0
constexpr int NKV = 32 + BIAS;
constexpr int NWG = 256, NTHR = 512;
#define RING_W 20
constexpr int W = RING_W;
constexpr unsigned WS_E = 0;
constexpr unsigned WS_QP = 4096;
constexpr unsigned WS_HP = WS_QP + 4 * 2 * 2048 * 16;
constexpr unsigned WS_ML = WS_HP + 4 * 2 * 1792 * 16;
constexpr unsigned WS_OP = WS_ML + 32 * 2 * 32 * 16;
constexpr unsigned WS_FP = WS_OP + 64 * 8 * 64 * 64;
constexpr unsigned WS_END = WS_FP + 2 * 128 * 64 * 64;
constexpr unsigned WS_STAMP = WS_END;
constexpr int L_XD = 0, L_RQ = 8192, L_RH = 12288, L_QS = 16384, L_HD = 18432, L_MX = 18944, L_LS = 19200, L_MRG = 19456,
              L_FIN = L_MRG + 32768, L_END = L_FIN + 32768;
constexpr unsigned SPIN_CAP = 1u << 16;
constexpr int AUX_NT = 2, AUX_SC1 = 16;
constexpr float SCALE = 0.08838834764831845f, LOG2E = 1.4426950408889634f;

__device__ __forceinline__ float bf2f(unsigned b) { return __builtin_bit_cast(float, b << 16); }
__device__ __forceinline__ float bfhi(unsigned b) { return __builtin_bit_cast(float, b & 0xffff0000u); }
__device__ __forceinline__ unsigned cvtpk(float lo, float hi) {
  return __builtin_bit_cast(unsigned, __builtin_convertvector(f32x2{lo, hi}, bf16x2));
}
__device__ __forceinline__ float rbf(float x) { return bfhi(cvtpk(x, x)); }
__device__ __forceinline__ f32x4 mfma(v4u a, v4u b, f32x4 c) {
  return __builtin_amdgcn_mfma_f32_16x16x32_bf16(__builtin_bit_cast(bf16x8, a), __builtin_bit_cast(bf16x8, b), c, 0, 0, 0);
}
template <int CTRL> __device__ __forceinline__ float dpp(float x) {
  return __builtin_bit_cast(float, __builtin_amdgcn_mov_dpp(__builtin_bit_cast(int, x), CTRL, 0xf, 0xf, true));
}
template <int CTRL> __device__ __forceinline__ unsigned dppu(unsigned x) {
  return (unsigned)__builtin_amdgcn_mov_dpp((int)x, CTRL, 0xf, 0xf, true);
}
__device__ __forceinline__ float x16_max(float x) {
  auto s = __builtin_amdgcn_permlane16_swap(__float_as_uint(x), __float_as_uint(x), false, false);
  return fmaxf(__uint_as_float(s[0]), __uint_as_float(s[1]));
}
__device__ __forceinline__ float x16_sum(float x) {
  auto s = __builtin_amdgcn_permlane16_swap(__float_as_uint(x), __float_as_uint(x), false, false);
  return __uint_as_float(s[0]) + __uint_as_float(s[1]);
}
__device__ __forceinline__ float x32_max(float x) {
  auto s = __builtin_amdgcn_permlane32_swap(__float_as_uint(x), __float_as_uint(x), false, false);
  return fmaxf(__uint_as_float(s[0]), __uint_as_float(s[1]));
}
__device__ __forceinline__ float x32_sum(float x) {
  auto s = __builtin_amdgcn_permlane32_swap(__float_as_uint(x), __float_as_uint(x), false, false);
  return __uint_as_float(s[0]) + __uint_as_float(s[1]);
}
__device__ __forceinline__ float row_sum(float x) {
  x += dpp<0xB1>(x); x += dpp<0x4E>(x); x += dpp<0x141>(x); x += dpp<0x140>(x); return x;
}
__device__ __forceinline__ float row_max(float x) {
  x = fmaxf(x, dpp<0xB1>(x)); x = fmaxf(x, dpp<0x4E>(x)); x = fmaxf(x, dpp<0x141>(x)); x = fmaxf(x, dpp<0x140>(x)); return x;
}
#define WG_BARRIER() do { asm volatile("s_waitcnt lgkmcnt(0)" ::: "memory"); __builtin_amdgcn_s_barrier(); asm volatile("" ::: "memory"); } while (0)
#define SB() __builtin_amdgcn_sched_barrier(0)
#define LDNT(rs, vo, so) __builtin_amdgcn_raw_buffer_load_b128(rs, vo, so, AUX_NT)
#define LDSC(rs, vo) __builtin_amdgcn_raw_buffer_load_b128(rs, vo, 0, AUX_SC1)
#define STSC(v, rs, vo) __builtin_amdgcn_raw_buffer_store_b128(v, rs, vo, 0, AUX_SC1)
__device__ __forceinline__ __amdgpu_buffer_rsrc_t mkrs(const void* p, unsigned bytes) {
  return __builtin_amdgcn_make_buffer_rsrc((void*)p, 0, (int)bytes, 0x00020000);
}
__device__ __forceinline__ bool tags_ok(v4u v, unsigned tag) { return v.y == tag && v.w == tag; }
__device__ __forceinline__ v4u gran(float a, float b, unsigned tag) {
  return v4u{__builtin_bit_cast(unsigned, a), tag, __builtin_bit_cast(unsigned, b), tag};
}
#define STAMP(i) do { } while (0)
#define STAMP_FLUSH() do { } while (0)

__device__ __forceinline__ void gemv64(const v4u (&wv)[4], const unsigned* xd  , const unsigned (&mk)[4],
                                       float* red  , int lane) {
  const int g = lane >> 4, bsel = (lane >> 3) & 1;
  f32x4 a0 = {0.f, 0.f, 0.f, 0.f}, a1 = {0.f, 0.f, 0.f, 0.f};
#pragma unroll
  for (int u = 0; u < 4; ++u) {
    const v2u x2 = *(const v2u*)(xd + bsel * 1024 + 8 * u + 2 * g);
    a0 = mfma(v4u{x2.x & mk[0], x2.x & mk[1], x2.x & mk[2], x2.x & mk[3]}, wv[u], a0);
    a1 = mfma(v4u{x2.y & mk[0], x2.y & mk[1], x2.y & mk[2], x2.y & mk[3]}, wv[u], a1);
  }
  f32x4 r;
#pragma unroll
  for (int i = 0; i < 4; ++i) { const float v = bsel ? a1[i] : a0[i]; r[i] = v + dpp<0x128>(v); }
  if (bsel == 0) *(f32x4*)(red + (g >> 1) * 64 + 8 * (lane & 7) + 4 * (g & 1)) = r;
}

__global__ void __launch_bounds__(NTHR, 2)
fused_decode(const u16* __restrict__ x, const u16* __restrict__ kc, const u16* __restrict__ vc, const u16* __restrict__ wq,
             const u16* __restrict__ wo, const u16* __restrict__ w1, const u16* __restrict__ w2, u16* __restrict__ out,
             unsigned char* ws) {
  __shared__ __attribute__((aligned(16))) unsigned char smem[L_END];
  const int tid = threadIdx.x, lane = tid & 63, w = __builtin_amdgcn_readfirstlane(tid >> 6);
  const int j = blockIdx.x;
  const int g = lane >> 4, n16 = lane & 15;
  STAMP(0);
  const __amdgpu_buffer_rsrc_t rs_ws = mkrs(ws, WS_END);
  const int ks = j & 63, ns = j >> 6;
  const bool has_ffn = ks < 56;
  const unsigned ep = __builtin_amdgcn_raw_buffer_load_b32(rs_ws, WS_E, 0, AUX_SC1);
  const __amdgpu_buffer_rsrc_t rs_x = mkrs(x, 4096);
  v4u xr[2];
  {
    const unsigned xo = (w < 2) ? (unsigned)(w * 2048 + lane * 32) : 0xFFFFFF00u;
    xr[0] = __builtin_amdgcn_raw_buffer_load_b128(rs_x, xo, 0, 0);
    xr[1] = __builtin_amdgcn_raw_buffer_load_b128(rs_x, xo, 16, 0);
  }
  const unsigned xres = __builtin_amdgcn_raw_buffer_load_b32(rs_x, (unsigned)((j >> 7) * 2048 + (j & 127) * 16 + (lane & 3) * 4), 0, 0);
  v4u tq[4], t1[4];
  {
    const __amdgpu_buffer_rsrc_t rs = mkrs(wq, 1024u * 8192u);
    const unsigned vo = (unsigned)(256 * ns + 32 * w + (lane >> 3)) * 8192u + ks * 128 + (lane & 7) * 16;
#pragma unroll
    for (int u = 0; u < 4; ++u) tq[u] = LDNT(rs, vo, u * 8 * 8192);
  }
  {
    const __amdgpu_buffer_rsrc_t rs = mkrs(w1, has_ffn ? 1024u * 7168u : 0u);
    const unsigned vo = (unsigned)(256 * ns + 32 * w + (lane >> 3)) * 7168u + ks * 128 + (lane & 7) * 16;
#pragma unroll
    for (int u = 0; u < 4; ++u) t1[u] = LDNT(rs, vo, u * 8 * 7168);
  }
  const int pair = j >> 5, sp = j & 31, bb = pair >> 2, kvh = pair & 3;
  const bool odd = sp & 1;
  const unsigned char* kbase = (const unsigned char*)kc + (size_t)pair * (8u << 20);
  const unsigned char* vbase = (const unsigned char*)vc + (size_t)pair * (8u << 20);
  const __amdgpu_buffer_rsrc_t rs_k = mkrs(kbase, 8u << 20), rs_v = mkrs(vbase, 8u << 20);
  const __amdgpu_buffer_rsrc_t rs_k2 = mkrs(kbase, odd ? 0u : (8u << 20)), rs_v2 = mkrs(vbase, odd ? 0u : (8u << 20));
  const unsigned wbase = (unsigned)(sp * 8 + w) * PAT_C * 1024u;
  const unsigned kvo = wbase + ((lane >> 2) & 3) * 256 + (4 * g + (lane & 3)) * 16;
  const unsigned vvo = wbase + lane * 16;
  const int st = pair * PAT_STG + (sp * 8 + w) * PAT_ROT, st2 = st + 8 * PAT_ROT;
#define UOFF(n, st_) ((unsigned)((((n) + (st_)) & 31) / PAT_C) * (256u * PAT_C * 1024u) + (unsigned)((((n) + (st_)) & 31) % PAT_C) * 1024u)
#define KVLD(rs, rs2, vo, i) ((i) < 32 - BIAS ? LDNT(rs, vo, UOFF(i, st)) : (i) < 32 ? LDNT(rs2, vo, UOFF(i, st)) : LDNT(rs2, vo, UOFF((i) - BIAS, st2) + 8u * PAT_C * 1024u))
#define ITEM(i) ((i) < NKV ? KVLD(rs_k, rs_k2, kvo, i) : KVLD(rs_v, rs_v2, vvo, (i) - NKV))
  v4u a[W];
#pragma unroll
  for (int i = 0; i < EARLY_K; ++i) a[i] = ITEM(i);
  SB();
  unsigned mk[4];
  {
    const int jp = lane & 7;
#pragma unroll
    for (int k = 0; k < 4; ++k) mk[k] = ((jp >> 1) == k) ? ((jp & 1) ? 0xffff0000u : 0x0000ffffu) : 0u;
  }
  if (w < 2) {
    float v[16];
#pragma unroll
    for (int k = 0; k < 4; ++k) { v[2 * k] = bf2f(xr[0][k]); v[2 * k + 1] = bfhi(xr[0][k]); v[8 + 2 * k] = bf2f(xr[1][k]); v[8 + 2 * k + 1] = bfhi(xr[1][k]); }
    float s = 0.f;
#pragma unroll
    for (int i = 0; i < 16; ++i) s += v[i];
    s = x32_sum(x16_sum(row_sum(s)));
    const float mean = s * (1.f / 1024.f);
    float q = 0.f;
#pragma unroll
    for (int i = 0; i < 16; ++i) { const float d = v[i] - mean; q += d * d; }
    q = x32_sum(x16_sum(row_sum(q)));
    const float var = rbf(q * (1.f / 1024.f));
    const float mb = rbf(mean);
    const float den = rbf(sqrtf(rbf(var + 1e-5f)));
    const float rden = 1.f / den;
    unsigned* xd = (unsigned*)(smem + L_XD) + w * 1024 + lane * 16;
#pragma unroll
    for (int k = 0; k < 4; ++k) {
      v4u o;
#pragma unroll
      for (int i = 0; i < 4; ++i) { const float t = rbf(v[4 * k + i] - mb) * rden; o[i] = cvtpk(t, t); }
      *(v4u*)(xd + 4 * k) = o;
    }
  }
  WG_BARRIER();
  STAMP(1);
  const unsigned tag = __builtin_amdgcn_readfirstlane(ep) + 1u;
  gemv64(tq, (const unsigned*)(smem + L_XD) + 256 * ns + 32 * w, mk, (float*)(smem + L_RQ) + w * 128, lane);
  WG_BARRIER();
  if (w == 0) {
    const float* red = (const float*)(smem + L_RQ) + (lane >> 5) * 64 + 2 * (lane & 31);
    f32x2 s = {0.f, 0.f};
#pragma unroll
    for (int ww = 0; ww < 8; ++ww) s += *(const f32x2*)(red + ww * 128);
    STSC(gran(s.x, s.y, tag), rs_ws, WS_QP + (unsigned)((ns * 2 + (lane >> 5)) * 2048 + ks * 32 + (lane & 31)) * 16u);
  }
  STAMP(2);
  gemv64(t1, (const unsigned*)(smem + L_XD) + 256 * ns + 32 * w, mk, (float*)(smem + L_RH) + w * 128, lane);
  WG_BARRIER();
  if (w == 1 && has_ffn) {
    const float* red = (const float*)(smem + L_RH) + (lane >> 5) * 64 + 2 * (lane & 31);
    f32x2 s = {0.f, 0.f};
#pragma unroll
    for (int ww = 0; ww < 8; ++ww) s += *(const f32x2*)(red + ww * 128);
    STSC(gran(s.x, s.y, tag), rs_ws, WS_HP + (unsigned)((ns * 2 + (lane >> 5)) * 1792 + ks * 32 + (lane & 31)) * 16u);
  }
  SB();
  asm volatile("s_waitcnt vmcnt(%0)" :: "n"(GWAIT) : "memory");
  SB();
  STAMP(3);
  {
    const unsigned qo = WS_QP + (unsigned)(bb * 2048 + kvh * 512 + tid) * 16u;
    v4u gq[4];
#pragma unroll
    for (int k = 0; k < 4; ++k) gq[k] = LDSC(rs_ws, qo + k * (2 * 2048 * 16));
    SB();
#pragma unroll
    for (int i = EARLY_K; i < W; ++i) a[i] = ITEM(i);
    SB();
    for (unsigned spins = 0;; ++spins) {
      const bool ok = tags_ok(gq[0], tag) && tags_ok(gq[1], tag) && tags_ok(gq[2], tag) && tags_ok(gq[3], tag);
      if (__all(ok) || spins > SPIN_CAP) break;
      __builtin_amdgcn_s_sleep(2); asm volatile("" :: "s"(ws) : "memory");
      if (!ok) {
#pragma unroll
        for (int k = 0; k < 4; ++k) gq[k] = LDSC(rs_ws, qo + k * (2 * 2048 * 16));
      }
    }
    const float q0 = (__uint_as_float(gq[0].x) + __uint_as_float(gq[1].x)) + (__uint_as_float(gq[2].x) + __uint_as_float(gq[3].x));
    const float q1 = (__uint_as_float(gq[0].z) + __uint_as_float(gq[1].z)) + (__uint_as_float(gq[2].z) + __uint_as_float(gq[3].z));
    ((unsigned*)(smem + L_QS))[tid] = cvtpk(q0, q1);
  }
  WG_BARRIER();
  STAMP(4);
  const int headn = 2 * (n16 & 3) + (n16 >> 3), bpp = (n16 >> 2) & 1;
  v4u qb[2];
#pragma unroll
  for (int t = 0; t < 2; ++t) qb[t] = *(const v4u*)(smem + L_QS + headn * 256 + (4 * g + 2 * t + bpp) * 16);
  const unsigned hobase = WS_HP + (unsigned)((lane >> 5) * 1792 + ks * 32 + (lane & 31)) * 16u;
  const bool hact = (w == 2 && has_ffn);
  v4u gh[4];
#pragma unroll
  for (int k = 0; k < 4; ++k) gh[k] = LDSC(rs_ws, hact ? hobase + k * (2 * 1792 * 16) : 0xFFFFFF00u);
  SB();
  float S[NKV];
#pragma unroll
  for (int i = 0; i < NKV; ++i) {
    const v4u cur = a[i % W];
    a[i % W] = ITEM(i + W);
    const f32x4 z = {0.f, 0.f, 0.f, 0.f};
    const f32x4 d0 = mfma(cur, qb[0], z), d1 = mfma(cur, qb[1], z);
    const float t1_ = d0[0] + d1[2], t2_ = d0[1] + d1[3];
    const float lo = t1_ + __builtin_bit_cast(float, __builtin_amdgcn_update_dpp(0, __builtin_bit_cast(int, t2_), 0x104, 0xf, 0x5, false));
    const float hi = t2_ + __builtin_bit_cast(float, __builtin_amdgcn_update_dpp(0, __builtin_bit_cast(int, t1_), 0x114, 0xf, 0xA, false));
    S[i] = bpp ? hi : lo;
    if (i >= 32 - BIAS && odd) S[i] = -INFINITY;
    SB();
  }
  STAMP(5);
  {
    float mx = S[0];
#pragma unroll
    for (int i = 1; i < NKV; ++i) mx = fmaxf(mx, S[i]);
    mx = x32_max(x16_max(mx));
    if (g == 0 && bpp == 0) ((float*)(smem + L_MX))[headn * 8 + w] = mx;
  }
  if (w == 2) {
    for (unsigned spins = 0;; ++spins) {
      const bool ok = !has_ffn || (tags_ok(gh[0], tag) && tags_ok(gh[1], tag) && tags_ok(gh[2], tag) && tags_ok(gh[3], tag));
      if (__all(ok) || spins > SPIN_CAP) break;
      __builtin_amdgcn_s_sleep(2); asm volatile("" :: "s"(ws) : "memory");
      if (!ok) {
#pragma unroll
        for (int k = 0; k < 4; ++k) gh[k] = LDSC(rs_ws, hobase + k * (2 * 1792 * 16));
      }
    }
    const float h0 = rbf((__uint_as_float(gh[0].x) + __uint_as_float(gh[1].x)) + (__uint_as_float(gh[2].x) + __uint_as_float(gh[3].x)));
    const float h1 = rbf((__uint_as_float(gh[0].z) + __uint_as_float(gh[1].z)) + (__uint_as_float(gh[2].z) + __uint_as_float(gh[3].z)));
    const float s0 = h0 / (1.f + __expf(-h0)), s1 = h1 / (1.f + __expf(-h1));
    v2u o; o.x = cvtpk(s0, s0); o.y = cvtpk(s1, s1);
    if (!has_ffn) { o.x = 0u; o.y = 0u; }
    *(v2u*)(smem + L_HD + ((lane >> 5) * 64 + 2 * (lane & 31)) * 4) = o;
  }
  WG_BARRIER();
  float mr;
  {
    const f32x4 m0 = *(const f32x4*)(smem + L_MX + headn * 32), m1 = *(const f32x4*)(smem + L_MX + headn * 32 + 16);
    const float M = fmaxf(fmaxf(fmaxf(m0[0], m0[1]), fmaxf(m0[2], m0[3])), fmaxf(fmaxf(m1[0], m1[1]), fmaxf(m1[2], m1[3])));
    mr = rbf(rbf(M) * SCALE);
  }
  unsigned P[NKV];
  float lsum = 0.f;
  {
    const float mc = mr * LOG2E;
#pragma unroll
    for (int i = 0; i < NKV; ++i) {
      const float sr = rbf(rbf(S[i]) * SCALE);
      const float p = __builtin_amdgcn_exp2f(sr * LOG2E - mc);
      lsum += p;
      P[i] = cvtpk(p, p);
    }
  }
  STAMP(6);
  f32x4 acc[4];
#pragma unroll
  for (int t = 0; t < 4; ++t) acc[t] = f32x4{0.f, 0.f, 0.f, 0.f};
#pragma unroll
  for (int i = NKV; i < 2 * NKV; ++i) {
    const v4u cur = a[i % W];
    if (i + W < 2 * NKV) a[i % W] = ITEM(i + W);
    const unsigned pp = P[i - NKV];
    const unsigned p0 = dppu<0x00>(pp), p1 = dppu<0x55>(pp), p2 = dppu<0xAA>(pp), p3 = dppu<0xFF>(pp);
    acc[0] = mfma(v4u{p0 & mk[0], p0 & mk[1], p0 & mk[2], p0 & mk[3]}, cur, acc[0]);
    acc[1] = mfma(v4u{p1 & mk[0], p1 & mk[1], p1 & mk[2], p1 & mk[3]}, cur, acc[1]);
    acc[2] = mfma(v4u{p2 & mk[0], p2 & mk[1], p2 & mk[2], p2 & mk[3]}, cur, acc[2]);
    acc[3] = mfma(v4u{p3 & mk[0], p3 & mk[1], p3 & mk[2], p3 & mk[3]}, cur, acc[3]);
    SB();
  }
  STAMP(7);
  lsum = x32_sum(x16_sum(lsum));
  if (g == 0 && bpp == 0) ((float*)(smem + L_LS))[headn * 8 + w] = lsum;
#pragma unroll
  for (int t = 0; t < 4; ++t) *(f32x4*)(smem + L_MRG + ((w * 4 + t) * 64 + lane) * 16) = acc[t];
  WG_BARRIER();
  {
    const int hl = w, d = 2 * lane;
    const int t = hl >> 1, hh = hl & 1, c = d >> 3, ap = 2 * hh + ((d >> 2) & 1), i0 = d & 3;
    const unsigned char* src = smem + L_MRG + (t * 64 + ap * 16 + c) * 16 + i0 * 4;
    f32x2 s = {0.f, 0.f};
#pragma unroll
    for (int ww = 0; ww < 8; ++ww) s += *(const f32x2*)(src + ww * 4096);
    const int ksp = (kvh * 8 + hl) * 2 + (d >> 6), wp = (d >> 3) & 7, pr = (d >> 1) & 3;
    STSC(gran(s.x, s.y, tag), rs_ws, WS_OP + (unsigned)((ksp * 8 + wp) * 64 + bb * 32 + sp) * 64u + pr * 16);
  }
  if (w == 0 && g == 0 && bpp == 0) {
    const f32x4 l0 = *(const f32x4*)(smem + L_LS + headn * 32), l1 = *(const f32x4*)(smem + L_LS + headn * 32 + 16);
    const float L = ((l0[0] + l0[1]) + (l0[2] + l0[3])) + ((l1[0] + l1[1]) + (l1[2] + l1[3]));
    STSC(gran(mr, L, tag), rs_ws, WS_ML + (unsigned)(((kvh * 8 + headn) * 2 + bb) * 32 + sp) * 16u);
  }
  SB();
  v4u tw2[4], two[4];
  {
    const __amdgpu_buffer_rsrc_t rs_w2 = mkrs(w2, has_ffn ? 3584u * 2048u : 0u);
    const __amdgpu_buffer_rsrc_t rs_wo = mkrs(wo, 4096u * 2048u);
    const unsigned wvo = (unsigned)(64 * ks + 8 * w + g) * 2048u + ns * 512 + n16 * 16;
#pragma unroll
    for (int i = 0; i < 4; ++i) tw2[i] = LDNT(rs_w2, wvo, (i >> 1) * 4 * 2048 + (i & 1) * 256);
#pragma unroll
    for (int i = 0; i < 4; ++i) two[i] = LDNT(rs_wo, wvo, (i >> 1) * 4 * 2048 + (i & 1) * 256);
  }
  SB();
  STAMP(8);
  f32x4 facc[2];
  {
    const unsigned* hd = (const unsigned*)(smem + L_HD) + ((lane >> 3) & 1) * 64 + 8 * w + g;
    const unsigned h0 = hd[0], h1 = hd[4];
    const v4u A0 = {h0 & mk[0], h0 & mk[1], h0 & mk[2], h0 & mk[3]}, A1 = {h1 & mk[0], h1 & mk[1], h1 & mk[2], h1 & mk[3]};
    facc[0] = mfma(A1, tw2[2], mfma(A0, tw2[0], f32x4{0.f, 0.f, 0.f, 0.f}));
    facc[1] = mfma(A1, tw2[3], mfma(A0, tw2[1], f32x4{0.f, 0.f, 0.f, 0.f}));
  }
  SB();
  {
    const unsigned mlo = WS_ML + (unsigned)((ks >> 1) * 64 + (lane >> 2)) * 16u;
    const unsigned opo = WS_OP + (unsigned)((ks * 8 + w) * 64) * 64u + lane * 16;
    v4u gm[4], go[4];
#pragma unroll
    for (int i = 0; i < 4; ++i) { gm[i] = LDSC(rs_ws, mlo + i * 256); go[i] = LDSC(rs_ws, opo + i * 1024); }
    for (unsigned spins = 0;; ++spins) {
      bool ok[4];
#pragma unroll
      for (int i = 0; i < 4; ++i) ok[i] = tags_ok(gm[i], tag) && tags_ok(go[i], tag);
      if (__all(ok[0] && ok[1] && ok[2] && ok[3]) || spins > SPIN_CAP) break;
      __builtin_amdgcn_s_sleep(POLL_SLEEP); asm volatile("" :: "s"(ws) : "memory");
#pragma unroll
      for (int i = 0; i < 4; ++i) if (!ok[i]) { gm[i] = LDSC(rs_ws, mlo + i * 256); go[i] = LDSC(rs_ws, opo + i * 1024); }
    }
    STAMP(9);
    float ov[2][2], rl[2];
#pragma unroll
    for (int b = 0; b < 2; ++b) {
      const float m0 = __uint_as_float(gm[2 * b].x), m1 = __uint_as_float(gm[2 * b + 1].x);
      float M = fmaxf(m0, m1);
      M = fmaxf(M, dpp<0x124>(M)); M = fmaxf(M, dpp<0x128>(M)); M = x32_max(x16_max(M));
      const float a0 = __builtin_amdgcn_exp2f((m0 - M) * LOG2E), a1 = __builtin_amdgcn_exp2f((m1 - M) * LOG2E);
      float L = a0 * __uint_as_float(gm[2 * b].z) + a1 * __uint_as_float(gm[2 * b + 1].z);
      float o0 = a0 * __uint_as_float(go[2 * b].x) + a1 * __uint_as_float(go[2 * b + 1].x);
      float o1 = a0 * __uint_as_float(go[2 * b].z) + a1 * __uint_as_float(go[2 * b + 1].z);
      L += dpp<0x124>(L); L += dpp<0x128>(L); L = x32_sum(x16_sum(L));
      o0 += dpp<0x124>(o0); o0 += dpp<0x128>(o0); o0 = x32_sum(x16_sum(o0));
      o1 += dpp<0x124>(o1); o1 += dpp<0x128>(o1); o1 = x32_sum(x16_sum(o1));
      rl[b] = 1.f / L;
      ov[b][0] = o0 * rl[b]; ov[b][1] = o1 * rl[b];
    }
    const bool bsel = (lane >> 3) & 1, csel = g & 1;
    const float v0 = csel ? ov[0][1] : ov[0][0], v1 = csel ? ov[1][1] : ov[1][0];
    const float vs = bsel ? v1 : v0;
    const unsigned pk = cvtpk(vs, vs);
    unsigned od0 = __builtin_amdgcn_update_dpp(0, (int)pk, 0x00, 0x3, 0xf, false);
    od0 = __builtin_amdgcn_update_dpp((int)od0, (int)pk, 0x55, 0xC, 0xf, false);
    unsigned od1 = __builtin_amdgcn_update_dpp(0, (int)pk, 0xAA, 0x3, 0xf, false);
    od1 = __builtin_amdgcn_update_dpp((int)od1, (int)pk, 0xFF, 0xC, 0xf, false);
    const v4u A0 = {od0 & mk[0], od0 & mk[1], od0 & mk[2], od0 & mk[3]}, A1 = {od1 & mk[0], od1 & mk[1], od1 & mk[2], od1 & mk[3]};
    const f32x4 r0 = mfma(A1, two[2], mfma(A0, two[0], facc[0]));
    const f32x4 r1 = mfma(A1, two[3], mfma(A0, two[1], facc[1]));
    *(f32x4*)(smem + L_FIN + ((w * 2 + 0) * 64 + lane) * 16) = r0;
    *(f32x4*)(smem + L_FIN + ((w * 2 + 1) * 64 + lane) * 16) = r1;
  }
  WG_BARRIER();
  if (tid < 256) {
    const int b = tid >> 7, cp = tid & 127, cc = 2 * cp;
    const int cb = cc >> 7, c = (cc & 127) >> 3, half = (cc >> 2) & 1, i0 = cc & 3;
    const unsigned char* src = smem + L_FIN + (cb * 64 + (2 * b + half) * 16 + c) * 16 + i0 * 4;
    f32x2 s = {0.f, 0.f};
#pragma unroll
    for (int ww = 0; ww < 8; ++ww) s += *(const f32x2*)(src + ww * 2048);
    const int cg = ns * 32 + (cc >> 3), pr = (cc >> 1) & 3;
    STSC(gran(s.x, s.y, tag), rs_ws, WS_FP + (unsigned)((b * 128 + cg) * 64 + ks) * 64u + pr * 16);
  }
  STAMP(10);
  if (w == 7) {
    const unsigned fo = WS_FP + (unsigned)j * (64 * 64) + lane * 16;
    for (int k = 0; k < FIN_DELAY; ++k) __builtin_amdgcn_s_sleep(8);
    v4u gf[4];
#pragma unroll
    for (int u = 0; u < 4; ++u) gf[u] = LDSC(rs_ws, fo + u * 1024);
    for (unsigned spins = 0;; ++spins) {
      bool ok[4];
#pragma unroll
      for (int u = 0; u < 4; ++u) ok[u] = tags_ok(gf[u], tag);
      if (__all(ok[0] && ok[1] && ok[2] && ok[3]) || spins > SPIN_CAP) break;
      __builtin_amdgcn_s_sleep(POLL_SLEEP); asm volatile("" :: "s"(ws) : "memory");
#pragma unroll
      for (int u = 0; u < 4; ++u) if (!ok[u]) gf[u] = LDSC(rs_ws, fo + u * 1024);
    }
    float s0 = 0.f, s1 = 0.f;
#pragma unroll
    for (int u = 0; u < 4; ++u) { s0 += __uint_as_float(gf[u].x); s1 += __uint_as_float(gf[u].z); }
    s0 += dpp<0x124>(s0); s0 += dpp<0x128>(s0); s0 = x32_sum(x16_sum(s0));
    s1 += dpp<0x124>(s1); s1 += dpp<0x128>(s1); s1 = x32_sum(x16_sum(s1));
    if (lane < 4) ((unsigned*)out)[(j >> 7) * 512 + (j & 127) * 4 + lane] = cvtpk(bf2f(xres) + s0, bfhi(xres) + s1);
    if (j == 0 && lane == 0) __builtin_amdgcn_raw_buffer_store_b32(tag, rs_ws, WS_E, 0, AUX_SC1);
    STAMP(11);
  }
  STAMP_FLUSH();
}

extern "C" void kernel_launch(void* const* d_in, const int* in_sizes, int n_in, void* d_out, int out_size, void* d_ws, size_t ws_size,
                              hipStream_t stream) {
  fused_decode<<<NWG, NTHR, 0, stream>>>((const u16*)d_in[0], (const u16*)d_in[1], (const u16*)d_in[2], (const u16*)d_in[3],
                                         (const u16*)d_in[4], (const u16*)d_in[6], (const u16*)d_in[7], (u16*)d_out,
                                         (unsigned char*)d_ws);
}
```

```cpp
#include <hip/hip_runtime.h>
#include <stdint.h>

typedef unsigned v4u __attribute__((ext_vector_type(4)));
typedef unsigned v2u __attribute__((ext_vector_type(2)));
typedef float f32x4 __attribute__((ext_vector_type(4)));
typedef float f32x2 __attribute__((ext_vector_type(2)));
typedef short bf16x8 __attribute__((ext_vector_type(8)));
typedef __bf16 bf16x2 __attribute__((ext_vector_type(2)));
typedef unsigned short u16;

#define PAT_C 4
#define PAT_STG 4
#define PAT_ROT 1

#define BIAS 0
#define TAU16 0
#define PACE_T0 110
#define CODE_PF 0
#define CODE_TRIM 4096u
#define FOLD 0
#define TAILMODE 1
#define QG_T 580
#define PACE_EVERY 4
#define W_IN_LOOP 3
#define OG_DELAY 100
#define FG_DELAY 70
#define ML_THR 56
#define MLPOLL_SLEEP 8
#define POLL_SLEEP 2
#define FIN_DELAY 2
constexpr int NKV = 32 + BIAS;
constexpr int NWG = 256, NTHR = 512;
#define RING_W 16
constexpr int W = RING_W;
constexpr unsigned WS_E = 0;
constexpr unsigned WS_QP = 4096;
constexpr unsigned WS_HP = WS_QP + 4 * 2 * 2048 * 16;
constexpr unsigned WS_ML = WS_HP + 4 * 2 * 1792 * 16;
constexpr unsigned WS_OP = WS_ML + 32 * 2 * 32 * 16;
constexpr unsigned WS_FP = WS_OP + 64 * 8 * 64 * 64;
constexpr unsigned WS_END = WS_FP + 2 * 128 * 64 * 64;
constexpr unsigned WS_STAMP = WS_END;
constexpr int L_XD = 0, L_RQ = 8192, L_RH = 12288, L_QS = 16384, L_HD = 18432, L_MX = 18944, L_LS = 19200, L_MRG = 19456,
              L_FIN = L_MRG + 32768, L_END = L_FIN + 32768;
constexpr unsigned SPIN_CAP = 1u << 16;
constexpr int AUX_NT = 2, AUX_SC1 = 16;
constexpr float SCALE = 0.08838834764831845f, LOG2E = 1.4426950408889634f;

__device__ __forceinline__ float bf2f(unsigned b) { return __builtin_bit_cast(float, b << 16); }
__device__ __forceinline__ float bfhi(unsigned b) { return __builtin_bit_cast(float, b & 0xffff0000u); }
__device__ __forceinline__ unsigned cvtpk(float lo, float hi) {
  return __builtin_bit_cast(unsigned, __builtin_convertvector(f32x2{lo, hi}, bf16x2));
}
__device__ __forceinline__ float rbf(float x) { return bfhi(cvtpk(x, x)); }
__device__ __forceinline__ f32x4 mfma(v4u a, v4u b, f32x4 c) {
  return __builtin_amdgcn_mfma_f32_16x16x32_bf16(__builtin_bit_cast(bf16x8, a), __builtin_bit_cast(bf16x8, b), c, 0, 0, 0);
}
template <int CTRL> __device__ __forceinline__ float dpp(float x) {
  return __builtin_bit_cast(float, __builtin_amdgcn_mov_dpp(__builtin_bit_cast(int, x), CTRL, 0xf, 0xf, true));
}
template <int CTRL> __device__ __forceinline__ unsigned dppu(unsigned x) {
  return (unsigned)__builtin_amdgcn_mov_dpp((int)x, CTRL, 0xf, 0xf, true);
}
__device__ __forceinline__ float x16_max(float x) {
  auto s = __builtin_amdgcn_permlane16_swap(__float_as_uint(x), __float_as_uint(x), false, false);
  return fmaxf(__uint_as_float(s[0]), __uint_as_float(s[1]));
}
__device__ __forceinline__ float x16_sum(float x) {
  auto s = __builtin_amdgcn_permlane16_swap(__float_as_uint(x), __float_as_uint(x), false, false);
  return __uint_as_float(s[0]) + __uint_as_float(s[1]);
}
__device__ __forceinline__ float x32_max(float x) {
  auto s = __builtin_amdgcn_permlane32_swap(__float_as_uint(x), __float_as_uint(x), false, false);
  return fmaxf(__uint_as_float(s[0]), __uint_as_float(s[1]));
}
__device__ __forceinline__ float x32_sum(float x) {
  auto s = __builtin_amdgcn_permlane32_swap(__float_as_uint(x), __float_as_uint(x), false, false);
  return __uint_as_float(s[0]) + __uint_as_float(s[1]);
}
__device__ __forceinline__ float row_sum(float x) {
  x += dpp<0xB1>(x); x += dpp<0x4E>(x); x += dpp<0x141>(x); x += dpp<0x140>(x); return x;
}
__device__ __forceinline__ float row_max(float x) {
  x = fmaxf(x, dpp<0xB1>(x)); x = fmaxf(x, dpp<0x4E>(x)); x = fmaxf(x, dpp<0x141>(x)); x = fmaxf(x, dpp<0x140>(x)); return x;
}
#define WG_BARRIER() do { asm volatile("s_waitcnt lgkmcnt(0)" ::: "memory"); __builtin_amdgcn_s_barrier(); asm volatile("" ::: "memory"); } while (0)
#define SB() __builtin_amdgcn_sched_barrier(0)
#define LDNT(rs, vo, so) __builtin_amdgcn_raw_buffer_load_b128(rs, vo, so, AUX_NT)
#define LDSC(rs, vo) __builtin_amdgcn_raw_buffer_load_b128(rs, vo, 0, AUX_SC1)
#define STSC(v, rs, vo) __builtin_amdgcn_raw_buffer_store_b128(v, rs, vo, 0, AUX_SC1)
__device__ __forceinline__ __amdgpu_buffer_rsrc_t mkrs(const void* p, unsigned bytes) {
  return __builtin_amdgcn_make_buffer_rsrc((void*)p, 0, (int)bytes, 0x00020000);
}
__device__ __forceinline__ bool tags_ok(v4u v, unsigned tag) { return (v.y == tag) & (v.w == tag); }
#define VM0() __builtin_amdgcn_s_waitcnt(0x0F70)
__device__ __forceinline__ v4u gran(float a, float b, unsigned tag) {
  return v4u{__builtin_bit_cast(unsigned, a), tag, __builtin_bit_cast(unsigned, b), tag};
}
#define STAMP(i) do { } while (0)
#define STAMP_FLUSH() do { } while (0)

__device__ __forceinline__ void gemv64(const v4u (&wv)[4], const unsigned* xd  , const unsigned (&mk)[4],
                                       float* red  , int lane) {
  const int g = lane >> 4, bsel = (lane >> 3) & 1;
  f32x4 a0 = {0.f, 0.f, 0.f, 0.f}, a1 = {0.f, 0.f, 0.f, 0.f};
#pragma unroll
  for (int u = 0; u < 4; ++u) {
    const v2u x2 = *(const v2u*)(xd + bsel * 1024 + 8 * u + 2 * g);
    a0 = mfma(v4u{x2.x & mk[0], x2.x & mk[1], x2.x & mk[2], x2.x & mk[3]}, wv[u], a0);
    a1 = mfma(v4u{x2.y & mk[0], x2.y & mk[1], x2.y & mk[2], x2.y & mk[3]}, wv[u], a1);
  }
  f32x4 r;
#pragma unroll
  for (int i = 0; i < 4; ++i) { const float v = bsel ? a1[i] : a0[i]; r[i] = v + dpp<0x128>(v); }
  if (bsel == 0) *(f32x4*)(red + (g >> 1) * 64 + 8 * (lane & 7) + 4 * (g & 1)) = r;
}

__global__ void __launch_bounds__(NTHR, 2)
fused_decode(const u16* __restrict__ x, const u16* __restrict__ kc, const u16* __restrict__ vc, const u16* __restrict__ wq,
             const u16* __restrict__ wo, const u16* __restrict__ w1, const u16* __restrict__ w2, u16* __restrict__ out,
             unsigned char* ws) {
  __shared__ __attribute__((aligned(16))) unsigned char smem[L_END];
  const int tid = threadIdx.x, lane = tid & 63, w = __builtin_amdgcn_readfirstlane(tid >> 6);
  const int j = blockIdx.x;
  const int g = lane >> 4, n16 = lane & 15;
  unsigned dbg_fail = 0; (void)dbg_fail;
  STAMP(0);
  const unsigned tstart = (unsigned)__builtin_amdgcn_s_memrealtime();
  const __amdgpu_buffer_rsrc_t rs_ws = mkrs(ws, WS_END);
  const int ks = j & 63, ns = j >> 6;
  const bool has_ffn = ks < 56;
  const unsigned ep = __builtin_amdgcn_raw_buffer_load_b32(rs_ws, WS_E, 0, AUX_SC1);
  const __amdgpu_buffer_rsrc_t rs_x = mkrs(x, 4096);
  v4u xr[2];
  {
    const unsigned xo = (w < 2) ? (unsigned)(w * 2048 + lane * 32) : 0xFFFFFF00u;
    xr[0] = __builtin_amdgcn_raw_buffer_load_b128(rs_x, xo, 0, 0);
    xr[1] = __builtin_amdgcn_raw_buffer_load_b128(rs_x, xo, 16, 0);
  }
  const unsigned xres = __builtin_amdgcn_raw_buffer_load_b32(rs_x, (unsigned)((j >> 7) * 2048 + (j & 127) * 16 + (lane & 3) * 4), 0, 0);
  v4u tq[4], t1[4];
  {
    const __amdgpu_buffer_rsrc_t rs = mkrs(wq, 1024u * 8192u);
    const unsigned vo = (unsigned)(256 * ns + 32 * w + (lane >> 3)) * 8192u + ks * 128 + (lane & 7) * 16;
#pragma unroll
    for (int u = 0; u < 4; ++u) tq[u] = LDNT(rs, vo, u * 8 * 8192);
  }
  {
    const __amdgpu_buffer_rsrc_t rs = mkrs(w1, has_ffn ? 1024u * 7168u : 0u);
    const unsigned vo = (unsigned)(256 * ns + 32 * w + (lane >> 3)) * 7168u + ks * 128 + (lane & 7) * 16;
#pragma unroll
    for (int u = 0; u < 4; ++u) t1[u] = LDNT(rs, vo, u * 8 * 7168);
  }
  const int pair = j >> 5, sp = j & 31, bb = pair >> 2, kvh = pair & 3;
  const bool odd = sp & 1;
  const unsigned char* kbase = (const unsigned char*)kc + (size_t)pair * (8u << 20);
  const unsigned char* vbase = (const unsigned char*)vc + (size_t)pair * (8u << 20);
  const __amdgpu_buffer_rsrc_t rs_k = mkrs(kbase, 8u << 20), rs_v = mkrs(vbase, 8u << 20);
  const __amdgpu_buffer_rsrc_t rs_k2 = mkrs(kbase, odd ? 0u : (8u << 20)), rs_v2 = mkrs(vbase, odd ? 0u : (8u << 20));
  const unsigned wbase = (unsigned)(sp * 8 + w) * PAT_C * 1024u;
  const unsigned kvo = wbase + ((lane >> 2) & 3) * 256 + (4 * g + (lane & 3)) * 16;
  const unsigned vvo = wbase + lane * 16;
  const int st = pair * PAT_STG + (sp * 8 + w) * PAT_ROT, st2 = st + 8 * PAT_ROT;
#define UOFF(n, st_) ((unsigned)((((n) + (st_)) & 31) / PAT_C) * (256u * PAT_C * 1024u) + (unsigned)((((n) + (st_)) & 31) % PAT_C) * 1024u)
#define KVLD(rs, rs2, vo, i) ((i) < 32 - BIAS ? LDNT(rs, vo, UOFF(i, st)) : (i) < 32 ? LDNT(rs2, vo, UOFF(i, st)) : LDNT(rs2, vo, UOFF((i) - BIAS, st2) + 8u * PAT_C * 1024u))
#define ITEM(i) ((i) < NKV ? KVLD(rs_k, rs_k2, kvo, i) : KVLD(rs_v, rs_v2, vvo, (i) - NKV))
#define WAIT_TICK(base, dt) do { unsigned d_; \
    asm volatile("1: s_memrealtime vcc\n\ts_waitcnt lgkmcnt(0)\n\ts_sub_u32 %0, vcc_lo, %1\n\ts_cmp_lt_u32 %0, %2\n\ts_cbranch_scc1 1b" \
                 : "=&s"(d_) : "s"(base), "n"(dt) : "vcc", "scc"); } while (0)
#define PACE(i) do { if (TAU16 && ((i) % PACE_EVERY) == 0) { unsigned d_; \
    asm volatile("1: s_memrealtime vcc\n\ts_waitcnt lgkmcnt(0)\n\ts_sub_u32 %0, vcc_lo, %1\n\ts_cmp_lt_u32 %0, %2\n\ts_cbranch_scc1 1b" \
                 : "=&s"(d_) : "s"(tstart), "n"(PACE_T0 + (((8 + (i)) * TAU16) >> 4)) : "vcc", "scc"); } } while (0)
  v4u a[W];
  SB();
  unsigned mk[4];
  {
    const int jp = lane & 7;
#pragma unroll
    for (int k = 0; k < 4; ++k) mk[k] = ((jp >> 1) == k) ? ((jp & 1) ? 0xffff0000u : 0x0000ffffu) : 0u;
  }
  if (w < 2) {
    float v[16];
#pragma unroll
    for (int k = 0; k < 4; ++k) { v[2 * k] = bf2f(xr[0][k]); v[2 * k + 1] = bfhi(xr[0][k]); v[8 + 2 * k] = bf2f(xr[1][k]); v[8 + 2 * k + 1] = bfhi(xr[1][k]); }
    float s = 0.f;
#pragma unroll
    for (int i = 0; i < 16; ++i) s += v[i];
    s = x32_sum(x16_sum(row_sum(s)));
    const float mean = s * (1.f / 1024.f);
    float q = 0.f;
#pragma unroll
    for (int i = 0; i < 16; ++i) { const float d = v[i] - mean; q += d * d; }
    q = x32_sum(x16_sum(row_sum(q)));
    const float var = rbf(q * (1.f / 1024.f));
    const float mb = rbf(mean);
    const float den = rbf(sqrtf(rbf(var + 1e-5f)));
    const float rden = 1.f / den;
    unsigned* xd = (unsigned*)(smem + L_XD) + w * 1024 + lane * 16;
#pragma unroll
    for (int k = 0; k < 4; ++k) {
      v4u o;
#pragma unroll
      for (int i = 0; i < 4; ++i) { const float t = rbf(v[4 * k + i] - mb) * rden; o[i] = cvtpk(t, t); }
      *(v4u*)(xd + 4 * k) = o;
    }
  }
  WG_BARRIER();
  STAMP(1);
  const unsigned tag = __builtin_amdgcn_readfirstlane(ep) + 1u;
  gemv64(tq, (const unsigned*)(smem + L_XD) + 256 * ns + 32 * w, mk, (float*)(smem + L_RQ) + w * 128, lane);
  WG_BARRIER();
  if (w == 0) {
    const float* red = (const float*)(smem + L_RQ) + (lane >> 5) * 64 + 2 * (lane & 31);
    f32x2 s = {0.f, 0.f};
#pragma unroll
    for (int ww = 0; ww < 8; ++ww) s += *(const f32x2*)(red + ww * 128);
    STSC(gran(s.x, s.y, tag), rs_ws, WS_QP + (unsigned)((ns * 2 + (lane >> 5)) * 2048 + ks * 32 + (lane & 31)) * 16u);
  }
  SB();
#pragma unroll
  for (int i = 0; i < 4; ++i) a[i] = ITEM(i);
  SB();
  STAMP(2);
  gemv64(t1, (const unsigned*)(smem + L_XD) + 256 * ns + 32 * w, mk, (float*)(smem + L_RH) + w * 128, lane);
  WG_BARRIER();
  if (w == 1 && has_ffn) {
    const float* red = (const float*)(smem + L_RH) + (lane >> 5) * 64 + 2 * (lane & 31);
    f32x2 s = {0.f, 0.f};
#pragma unroll
    for (int ww = 0; ww < 8; ++ww) s += *(const f32x2*)(red + ww * 128);
    STSC(gran(s.x, s.y, tag), rs_ws, WS_HP + (unsigned)((ns * 2 + (lane >> 5)) * 1792 + ks * 32 + (lane & 31)) * 16u);
  }
  SB();
#pragma unroll
  for (int i = 4; i < 8; ++i) a[i] = ITEM(i);
  SB();
  asm volatile("s_waitcnt vmcnt(4)" ::: "memory");
  STAMP(3);
  {
    const unsigned qo = WS_QP + (unsigned)(bb * 2048 + kvh * 512 + tid) * 16u;
    v4u gq[4];
    WAIT_TICK(tstart, QG_T);
#pragma unroll
    for (int k = 0; k < 4; ++k) gq[k] = LDSC(rs_ws, qo + k * (2 * 2048 * 16));
    SB();
#pragma unroll
    for (int i = 8; i < 12; ++i) a[i] = ITEM(i);
    SB();
    asm volatile("s_waitcnt vmcnt(8)" ::: "memory");
    SB();
#pragma unroll
    for (int i = 12; i < 16; ++i) a[i] = ITEM(i);
    SB();
    {
      bool ok = tags_ok(gq[0], tag) & tags_ok(gq[1], tag) & tags_ok(gq[2], tag) & tags_ok(gq[3], tag);
      if (!__all(ok)) {
        unsigned spins = 0;
        do {
          __builtin_amdgcn_s_sleep(2); asm volatile("" :: "s"(ws) : "memory");
          if (!ok) {
#pragma unroll
            for (int k = 0; k < 4; ++k) gq[k] = LDSC(rs_ws, qo + k * (2 * 2048 * 16));
          }
          VM0();
          ok = tags_ok(gq[0], tag) & tags_ok(gq[1], tag) & tags_ok(gq[2], tag) & tags_ok(gq[3], tag);
        } while (!__all(ok) && ++spins < SPIN_CAP);
      }
    }
    const float q0 = (__uint_as_float(gq[0].x) + __uint_as_float(gq[1].x)) + (__uint_as_float(gq[2].x) + __uint_as_float(gq[3].x));
    const float q1 = (__uint_as_float(gq[0].z) + __uint_as_float(gq[1].z)) + (__uint_as_float(gq[2].z) + __uint_as_float(gq[3].z));
    ((unsigned*)(smem + L_QS))[tid] = cvtpk(q0, q1);
  }
  WG_BARRIER();
  STAMP(4);
#pragma unroll
  for (int i = 16; i < W; ++i) a[i] = ITEM(i);
  SB();
  const int headn = 2 * (n16 & 3) + (n16 >> 3), bpp = (n16 >> 2) & 1;
  v4u qb[2];
#pragma unroll
  for (int t = 0; t < 2; ++t) qb[t] = *(const v4u*)(smem + L_QS + headn * 256 + (4 * g + 2 * t + bpp) * 16);
  const unsigned hobase = WS_HP + (unsigned)((lane >> 5) * 1792 + ks * 32 + (lane & 31)) * 16u;
  const bool hact = (w == 2 && has_ffn);
  v4u gh[4];
#pragma unroll
  for (int k = 0; k < 4; ++k) gh[k] = LDSC(rs_ws, hact ? hobase + k * (2 * 1792 * 16) : 0xFFFFFF00u);
  SB();
  float S[NKV];
#pragma unroll
  for (int i = 0; i < NKV; ++i) {
    const v4u cur = a[i % W];
    PACE(i + W);
    a[i % W] = ITEM(i + W);
    const f32x4 z = {0.f, 0.f, 0.f, 0.f};
    const f32x4 d0 = mfma(cur, qb[0], z), d1 = mfma(cur, qb[1], z);
    const float t1_ = d0[0] + d1[2], t2_ = d0[1] + d1[3];
    const float lo = t1_ + __builtin_bit_cast(float, __builtin_amdgcn_update_dpp(0, __builtin_bit_cast(int, t2_), 0x104, 0xf, 0x5, false));
    const float hi = t2_ + __builtin_bit_cast(float, __builtin_amdgcn_update_dpp(0, __builtin_bit_cast(int, t1_), 0x114, 0xf, 0xA, false));
    S[i] = bpp ? hi : lo;
    if (i >= 32 - BIAS && odd) S[i] = -INFINITY;
    SB();
  }
  STAMP(5);
  {
    float mx = S[0];
#pragma unroll
    for (int i = 1; i < NKV; ++i) mx = fmaxf(mx, S[i]);
    mx = x32_max(x16_max(mx));
    if (g == 0 && bpp == 0) ((float*)(smem + L_MX))[headn * 8 + w] = mx;
  }
  if (w == 2) {
    bool ok = !has_ffn | (tags_ok(gh[0], tag) & tags_ok(gh[1], tag) & tags_ok(gh[2], tag) & tags_ok(gh[3], tag));
    if (!__all(ok)) {
      unsigned spins = 0;
      do {
        __builtin_amdgcn_s_sleep(2); asm volatile("" :: "s"(ws) : "memory");
        if (!ok) {
#pragma unroll
          for (int k = 0; k < 4; ++k) gh[k] = LDSC(rs_ws, hobase + k * (2 * 1792 * 16));
        }
        VM0();
        ok = !has_ffn | (tags_ok(gh[0], tag) & tags_ok(gh[1], tag) & tags_ok(gh[2], tag) & tags_ok(gh[3], tag));
      } while (!__all(ok) && ++spins < SPIN_CAP);
    }
    const float h0 = rbf((__uint_as_float(gh[0].x) + __uint_as_float(gh[1].x)) + (__uint_as_float(gh[2].x) + __uint_as_float(gh[3].x)));
    const float h1 = rbf((__uint_as_float(gh[0].z) + __uint_as_float(gh[1].z)) + (__uint_as_float(gh[2].z) + __uint_as_float(gh[3].z)));
    const float s0 = h0 / (1.f + __expf(-h0)), s1 = h1 / (1.f + __expf(-h1));
    v2u o; o.x = cvtpk(s0, s0); o.y = cvtpk(s1, s1);
    if (!has_ffn) { o.x = 0u; o.y = 0u; }
    *(v2u*)(smem + L_HD + ((lane >> 5) * 64 + 2 * (lane & 31)) * 4) = o;
  }
  WG_BARRIER();
  float mr;
  {
    const f32x4 m0 = *(const f32x4*)(smem + L_MX + headn * 32), m1 = *(const f32x4*)(smem + L_MX + headn * 32 + 16);
    const float M = fmaxf(fmaxf(fmaxf(m0[0], m0[1]), fmaxf(m0[2], m0[3])), fmaxf(fmaxf(m1[0], m1[1]), fmaxf(m1[2], m1[3])));
    mr = rbf(rbf(M) * SCALE);
  }
  v4u tw[8];
  const __amdgpu_buffer_rsrc_t rs_w2 = mkrs(w2, has_ffn ? 3584u * 2048u : 0u);
  const __amdgpu_buffer_rsrc_t rs_wo = mkrs(wo, 4096u * 2048u);
  const unsigned wvo = (unsigned)(64 * ks + 8 * w + g) * 2048u + ns * 512 + n16 * 16;
#define WLD(k) ((k) < 4 ? LDNT(rs_w2, wvo, (((k) >> 1) & 1) * 4 * 2048 + ((k) & 1) * 256) : LDNT(rs_wo, wvo, (((k) >> 1) & 1) * 4 * 2048 + ((k) & 1) * 256))
  float lsum = 0.f;
  const float mc = mr * LOG2E;
  unsigned P[NKV];
#pragma unroll
  for (int i = 0; i < NKV; ++i) {
    const float sr = rbf(rbf(S[i]) * SCALE);
    const float p = __builtin_amdgcn_exp2f(sr * LOG2E - mc);
    lsum += p;
    P[i] = cvtpk(p, p);
  }
  SB();
  f32x4 acc[4];
#pragma unroll
  for (int t = 0; t < 4; ++t) acc[t] = f32x4{0.f, 0.f, 0.f, 0.f};
#pragma unroll
  for (int i = NKV; i < 2 * NKV; ++i) {
    const v4u cur = a[i % W];
    if (i + W < 2 * NKV) { PACE(i + W); a[i % W] = ITEM(i + W); }
    if (i >= 2 * NKV - W_IN_LOOP) { if (i == 2 * NKV - W_IN_LOOP) PACE(2 * NKV); tw[i - (2 * NKV - W_IN_LOOP)] = WLD(i - (2 * NKV - W_IN_LOOP)); }
    const unsigned pp = P[i - NKV];
    const unsigned p0 = dppu<0x00>(pp), p1 = dppu<0x55>(pp), p2 = dppu<0xAA>(pp), p3 = dppu<0xFF>(pp);
    acc[0] = mfma(v4u{p0 & mk[0], p0 & mk[1], p0 & mk[2], p0 & mk[3]}, cur, acc[0]);
    acc[1] = mfma(v4u{p1 & mk[0], p1 & mk[1], p1 & mk[2], p1 & mk[3]}, cur, acc[1]);
    acc[2] = mfma(v4u{p2 & mk[0], p2 & mk[1], p2 & mk[2], p2 & mk[3]}, cur, acc[2]);
    acc[3] = mfma(v4u{p3 & mk[0], p3 & mk[1], p3 & mk[2], p3 & mk[3]}, cur, acc[3]);
    SB();
  }
  STAMP(7);
  lsum = x32_sum(x16_sum(lsum));
  if (g == 0 && bpp == 0) ((float*)(smem + L_LS))[headn * 8 + w] = lsum;
#pragma unroll
  for (int t = 0; t < 4; ++t) *(f32x4*)(smem + L_MRG + ((w * 4 + t) * 64 + lane) * 16) = acc[t];
  WG_BARRIER();
  STAMP(8);
  {
    const int hl = w, d = 2 * lane;
    const int t = hl >> 1, hh = hl & 1, c = d >> 3, ap = 2 * hh + ((d >> 2) & 1), i0 = d & 3;
    const unsigned char* src = smem + L_MRG + (t * 64 + ap * 16 + c) * 16 + i0 * 4;
    f32x2 s = {0.f, 0.f};
#pragma unroll
    for (int ww = 0; ww < 8; ++ww) s += *(const f32x2*)(src + ww * 4096);
    const int ksp = (kvh * 8 + hl) * 2 + (d >> 6), wp = (d >> 3) & 7, pr = (d >> 1) & 3;
    STSC(gran(s.x, s.y, tag), rs_ws, WS_OP + (unsigned)((ksp * 8 + wp) * 64 + bb * 32 + sp) * 64u + pr * 16);
  }
  if (w == 0 && g == 0 && bpp == 0) {
    const f32x4 l0 = *(const f32x4*)(smem + L_LS + headn * 32), l1 = *(const f32x4*)(smem + L_LS + headn * 32 + 16);
    const float L = ((l0[0] + l0[1]) + (l0[2] + l0[3])) + ((l1[0] + l1[1]) + (l1[2] + l1[3]));
    STSC(gran(mr, L, tag), rs_ws, WS_ML + (unsigned)(((kvh * 8 + headn) * 2 + bb) * 32 + sp) * 16u);
  }
  SB();
  STAMP(9);
  const unsigned tpub = (unsigned)__builtin_amdgcn_s_memrealtime();
#pragma unroll
  for (int k = W_IN_LOOP; k < 8; ++k) tw[k] = WLD(k);
  SB();
  STAMP(10);
  f32x4 facc[2];
  {
    const unsigned* hd = (const unsigned*)(smem + L_HD) + ((lane >> 3) & 1) * 64 + 8 * w + g;
    const unsigned h0 = hd[0], h1 = hd[4];
    const v4u A0 = {h0 & mk[0], h0 & mk[1], h0 & mk[2], h0 & mk[3]}, A1 = {h1 & mk[0], h1 & mk[1], h1 & mk[2], h1 & mk[3]};
    facc[0] = mfma(A1, tw[2], mfma(A0, tw[0], f32x4{0.f, 0.f, 0.f, 0.f}));
    facc[1] = mfma(A1, tw[3], mfma(A0, tw[1], f32x4{0.f, 0.f, 0.f, 0.f}));
  }
  asm volatile("" :: "v"(facc[0]), "v"(facc[1]));
  SB();
  STAMP(11);
  WAIT_TICK(tpub, OG_DELAY);
  STAMP(12);
  {
    const unsigned mlo = WS_ML + (unsigned)((ks >> 1) * 64 + (lane >> 2)) * 16u;
    const unsigned opo = WS_OP + (unsigned)((ks * 8 + w) * 64) * 64u + lane * 16;
    v4u gm[4], go[4];
#pragma unroll
    for (int i = 0; i < 4; ++i) { gm[i] = LDSC(rs_ws, mlo + i * 256); go[i] = LDSC(rs_ws, opo + i * 1024); }
    {
      bool ok[4];
#pragma unroll
      for (int i = 0; i < 4; ++i) ok[i] = tags_ok(gm[i], tag) & tags_ok(go[i], tag);
      if (!__all(ok[0] & ok[1] & ok[2] & ok[3])) {
        unsigned spins = 0;
        do {
          __builtin_amdgcn_s_sleep(POLL_SLEEP); asm volatile("" :: "s"(ws) : "memory");
#pragma unroll
          for (int i = 0; i < 4; ++i) if (!ok[i]) { gm[i] = LDSC(rs_ws, mlo + i * 256); go[i] = LDSC(rs_ws, opo + i * 1024); }
          VM0();
#pragma unroll
          for (int i = 0; i < 4; ++i) ok[i] = tags_ok(gm[i], tag) & tags_ok(go[i], tag);
        } while (!__all(ok[0] & ok[1] & ok[2] & ok[3]) && ++spins < SPIN_CAP);
      }
    }
    STAMP(13);
    float ov[2][2], rl[2];
#pragma unroll
    for (int b = 0; b < 2; ++b) {
      const float m0 = __uint_as_float(gm[2 * b].x), m1 = __uint_as_float(gm[2 * b + 1].x);
      float M = fmaxf(m0, m1);
      M = fmaxf(M, dpp<0x124>(M)); M = fmaxf(M, dpp<0x128>(M)); M = x32_max(x16_max(M));
      const float a0 = __builtin_amdgcn_exp2f((m0 - M) * LOG2E), a1 = __builtin_amdgcn_exp2f((m1 - M) * LOG2E);
      float L = a0 * __uint_as_float(gm[2 * b].z) + a1 * __uint_as_float(gm[2 * b + 1].z);
      float o0 = a0 * __uint_as_float(go[2 * b].x) + a1 * __uint_as_float(go[2 * b + 1].x);
      float o1 = a0 * __uint_as_float(go[2 * b].z) + a1 * __uint_as_float(go[2 * b + 1].z);
      L += dpp<0x124>(L); L += dpp<0x128>(L); L = x32_sum(x16_sum(L));
      o0 += dpp<0x124>(o0); o0 += dpp<0x128>(o0); o0 = x32_sum(x16_sum(o0));
      o1 += dpp<0x124>(o1); o1 += dpp<0x128>(o1); o1 = x32_sum(x16_sum(o1));
      rl[b] = 1.f / L;
      ov[b][0] = o0 * rl[b]; ov[b][1] = o1 * rl[b];
    }
    const bool bsel = (lane >> 3) & 1, csel = g & 1;
    const float v0 = csel ? ov[0][1] : ov[0][0], v1 = csel ? ov[1][1] : ov[1][0];
    const float vs = bsel ? v1 : v0;
    const unsigned pk = cvtpk(vs, vs);
    unsigned od0 = __builtin_amdgcn_update_dpp(0, (int)pk, 0x00, 0x3, 0xf, false);
    od0 = __builtin_amdgcn_update_dpp((int)od0, (int)pk, 0x55, 0xC, 0xf, false);
    unsigned od1 = __builtin_amdgcn_update_dpp(0, (int)pk, 0xAA, 0x3, 0xf, false);
    od1 = __builtin_amdgcn_update_dpp((int)od1, (int)pk, 0xFF, 0xC, 0xf, false);
    const v4u A0 = {od0 & mk[0], od0 & mk[1], od0 & mk[2], od0 & mk[3]}, A1 = {od1 & mk[0], od1 & mk[1], od1 & mk[2], od1 & mk[3]};
    const f32x4 r0 = mfma(A1, tw[6], mfma(A0, tw[4], facc[0]));
    const f32x4 r1 = mfma(A1, tw[7], mfma(A0, tw[5], facc[1]));
    *(f32x4*)(smem + L_FIN + ((w * 2 + 0) * 64 + lane) * 16) = r0;
    *(f32x4*)(smem + L_FIN + ((w * 2 + 1) * 64 + lane) * 16) = r1;
  }
  WG_BARRIER();
  if (tid < 256) {
    const int b = tid >> 7, cp = tid & 127, cc = 2 * cp;
    const int cb = cc >> 7, c = (cc & 127) >> 3, half = (cc >> 2) & 1, i0 = cc & 3;
    const unsigned char* src = smem + L_FIN + (cb * 64 + (2 * b + half) * 16 + c) * 16 + i0 * 4;
    f32x2 s = {0.f, 0.f};
#pragma unroll
    for (int ww = 0; ww < 8; ++ww) s += *(const f32x2*)(src + ww * 2048);
    const int cg = ns * 32 + (cc >> 3), pr = (cc >> 1) & 3;
    STSC(gran(s.x, s.y, tag), rs_ws, WS_FP + (unsigned)((b * 128 + cg) * 64 + ks) * 64u + pr * 16);
  }
  STAMP(14);
  if (w == 7) {
    const unsigned fo = WS_FP + (unsigned)j * (64 * 64) + lane * 16;
    { const unsigned tf = (unsigned)__builtin_amdgcn_s_memrealtime(); WAIT_TICK(tf, FG_DELAY); }
    v4u gf[4];
#pragma unroll
    for (int u = 0; u < 4; ++u) gf[u] = LDSC(rs_ws, fo + u * 1024);
    {
      bool ok[4];
#pragma unroll
      for (int u = 0; u < 4; ++u) ok[u] = tags_ok(gf[u], tag);
      if (!__all(ok[0] & ok[1] & ok[2] & ok[3])) {
        unsigned spins = 0;
        do {
          __builtin_amdgcn_s_sleep(POLL_SLEEP); asm volatile("" :: "s"(ws) : "memory");
#pragma unroll
          for (int u = 0; u < 4; ++u) if (!ok[u]) gf[u] = LDSC(rs_ws, fo + u * 1024);
          VM0();
#pragma unroll
          for (int u = 0; u < 4; ++u) ok[u] = tags_ok(gf[u], tag);
        } while (!__all(ok[0] & ok[1] & ok[2] & ok[3]) && ++spins < SPIN_CAP);
      }
    }
    float s0 = 0.f, s1 = 0.f;
#pragma unroll
    for (int u = 0; u < 4; ++u) { s0 += __uint_as_float(gf[u].x); s1 += __uint_as_float(gf[u].z); }
    s0 += dpp<0x124>(s0); s0 += dpp<0x128>(s0); s0 = x32_sum(x16_sum(s0));
    s1 += dpp<0x124>(s1); s1 += dpp<0x128>(s1); s1 = x32_sum(x16_sum(s1));
    if (lane < 4) ((unsigned*)out)[(j >> 7) * 512 + (j & 127) * 4 + lane] = cvtpk(bf2f(xres) + s0, bfhi(xres) + s1);
    if (j == 0 && lane == 0) __builtin_amdgcn_raw_buffer_store_b32(tag, rs_ws, WS_E, 0, AUX_SC1);
    STAMP(15);
  }
  STAMP_FLUSH();
}

extern "C" void kernel_launch(void* const* d_in, const int* in_sizes, int n_in, void* d_out, int out_size, void* d_ws, size_t ws_size,
                              hipStream_t stream) {
  fused_decode<<<NWG, NTHR, 0, stream>>>((const u16*)d_in[0], (const u16*)d_in[1], (const u16*)d_in[2], (const u16*)d_in[3],
                                         (const u16*)d_in[4], (const u16*)d_in[6], (const u16*)d_in[7], (u16*)d_out,
                                         (unsigned char*)d_ws);
}
```

```cpp
#include <hip/hip_runtime.h>
#include <stdint.h>

typedef unsigned v4u __attribute__((ext_vector_type(4)));
typedef unsigned v2u __attribute__((ext_vector_type(2)));
typedef float f32x4 __attribute__((ext_vector_type(4)));
typedef float f32x2 __attribute__((ext_vector_type(2)));
typedef short bf16x8 __attribute__((ext_vector_type(8)));
typedef __bf16 bf16x2 __attribute__((ext_vector_type(2)));
typedef unsigned short u16;

#define PAT_C 4
#define PAT_STG 4
#define PAT_ROT 1

#define FINE 0
#define PSLEEP "1"
#define YPRIO 0
#define ABL 0
#define BIAS 0
#define TAU16 0
#define PACE_P0 860
#define CODE_PF 0
#define CODE_TRIM 4096u
#define FOLD 0
#define TAILMODE 1
#define ML_PUB_STEP 4
#define ML_GET_STEP 14
#define ML_USE_STEP 26
#define POLL_HALF 10
#define EARLY_N 8
#define SYNC_MID 1
#define H_USE_STEP 8
#define POLL_ROUNDS 4
#define OP_PAD 256
#define FP_PAD 256
#define QG_T 560
#define PACE_EVERY 4
#define W_IN_LOOP 3
#define OG_DELAY 160
#define FG_DELAY 70
#define ML_THR 56
#define MLPOLL_SLEEP 8
#define POLL_SLEEP 2
#define FIN_DELAY 2
constexpr int NKV = 32 + BIAS;
constexpr int NWG = 256, NTHR = 512;
#define RING_W 20
constexpr int W = RING_W;
#define RING_D 8
constexpr int RD = RING_D;
constexpr int TOPUP = (RING_W - RING_D) < 8 ? (RING_W - RING_D) : 8;
constexpr unsigned WS_E = 0;
constexpr unsigned WS_QP = 4096;
constexpr unsigned WS_HP = WS_QP + 4 * 2 * 2048 * 16;
constexpr unsigned WS_TS = WS_HP + 4 * 2 * 1792 * 16;
constexpr unsigned WS_ML = WS_TS + 256 * 16;
constexpr unsigned OP_STR = 8192 + OP_PAD, FP_STR = 4096 + FP_PAD;
constexpr unsigned WS_OP = WS_ML + 32 * 2 * 32 * 16;
constexpr unsigned WS_FP = WS_OP + 256 * OP_STR;
constexpr unsigned WS_END = WS_FP + 256 * FP_STR;
constexpr unsigned WS_STAMP = WS_END;
constexpr int L_XD = 0, L_RQ = 8192, L_RH = 12288, L_QS = 16384, L_HD = 18432, L_MX = 18944, L_LS = 19200, L_FS = 19456, L_MISC = 19712, L_MRG = 19776,
              L_FIN = L_MRG + 32768, L_END = L_FIN + 32768;
constexpr unsigned SPIN_CAP = 1u << 16;
constexpr int AUX_NT = 2, AUX_SC1 = 16;
constexpr float SCALE = 0.08838834764831845f, LOG2E = 1.4426950408889634f;

__device__ __forceinline__ float bf2f(unsigned b) { return __builtin_bit_cast(float, b << 16); }
__device__ __forceinline__ float bfhi(unsigned b) { return __builtin_bit_cast(float, b & 0xffff0000u); }
__device__ __forceinline__ unsigned cvtpk(float lo, float hi) {
  return __builtin_bit_cast(unsigned, __builtin_convertvector(f32x2{lo, hi}, bf16x2));
}
__device__ __forceinline__ float rbf(float x) { return bfhi(cvtpk(x, x)); }
__device__ __forceinline__ f32x4 mfma(v4u a, v4u b, f32x4 c) {
  return __builtin_amdgcn_mfma_f32_16x16x32_bf16(__builtin_bit_cast(bf16x8, a), __builtin_bit_cast(bf16x8, b), c, 0, 0, 0);
}
typedef short s16x4 __attribute__((ext_vector_type(4)));
__device__ __forceinline__ f32x4 mfma16(unsigned a0, unsigned a1, unsigned b0, unsigned b1, f32x4 c) {
  return __builtin_amdgcn_mfma_f32_16x16x16bf16_1k(__builtin_bit_cast(s16x4, v2u{a0, a1}), __builtin_bit_cast(s16x4, v2u{b0, b1}), c, 0, 0, 0);
}
template <int CTRL> __device__ __forceinline__ float dpp(float x) {
  return __builtin_bit_cast(float, __builtin_amdgcn_mov_dpp(__builtin_bit_cast(int, x), CTRL, 0xf, 0xf, true));
}
template <int CTRL> __device__ __forceinline__ unsigned dppu(unsigned x) {
  return (unsigned)__builtin_amdgcn_mov_dpp((int)x, CTRL, 0xf, 0xf, true);
}
__device__ __forceinline__ float x16_max(float x) {
  auto s = __builtin_amdgcn_permlane16_swap(__float_as_uint(x), __float_as_uint(x), false, false);
  return fmaxf(__uint_as_float(s[0]), __uint_as_float(s[1]));
}
__device__ __forceinline__ float x16_sum(float x) {
  auto s = __builtin_amdgcn_permlane16_swap(__float_as_uint(x), __float_as_uint(x), false, false);
  return __uint_as_float(s[0]) + __uint_as_float(s[1]);
}
__device__ __forceinline__ float x32_max(float x) {
  auto s = __builtin_amdgcn_permlane32_swap(__float_as_uint(x), __float_as_uint(x), false, false);
  return fmaxf(__uint_as_float(s[0]), __uint_as_float(s[1]));
}
__device__ __forceinline__ float x32_sum(float x) {
  auto s = __builtin_amdgcn_permlane32_swap(__float_as_uint(x), __float_as_uint(x), false, false);
  return __uint_as_float(s[0]) + __uint_as_float(s[1]);
}
__device__ __forceinline__ float row_sum(float x) {
  x += dpp<0xB1>(x); x += dpp<0x4E>(x); x += dpp<0x141>(x); x += dpp<0x140>(x); return x;
}
__device__ __forceinline__ float row_max(float x) {
  x = fmaxf(x, dpp<0xB1>(x)); x = fmaxf(x, dpp<0x4E>(x)); x = fmaxf(x, dpp<0x141>(x)); x = fmaxf(x, dpp<0x140>(x)); return x;
}
#define WG_BARRIER() do { asm volatile("s_waitcnt lgkmcnt(0)" ::: "memory"); __builtin_amdgcn_s_barrier(); asm volatile("" ::: "memory"); } while (0)
#define SB() __builtin_amdgcn_sched_barrier(0)
#define LAS __attribute__((address_space(3)))
#define WAIT_WAVES() do { unsigned n_ = 0; while (__hip_atomic_load((LAS unsigned*)(smem + L_MISC + 4), __ATOMIC_RELAXED, __HIP_MEMORY_SCOPE_WORKGROUP) < 8u && ++n_ < SPIN_CAP) __builtin_amdgcn_s_sleep(1); asm volatile("" ::: "memory"); } while (0)
#define LDNT(rs, vo, so) __builtin_amdgcn_raw_buffer_load_b128(rs, vo, so, AUX_NT)
#define LDSC(rs, vo) __builtin_amdgcn_raw_buffer_load_b128(rs, vo, 0, AUX_SC1)
#define STSC(v, rs, vo) __builtin_amdgcn_raw_buffer_store_b128(v, rs, vo, 0, AUX_SC1)
__device__ __forceinline__ __amdgpu_buffer_rsrc_t mkrs(const void* p, unsigned bytes) {
  return __builtin_amdgcn_make_buffer_rsrc((void*)p, 0, (int)bytes, 0x00020000);
}
__device__ __forceinline__ bool tags_ok(v4u v, unsigned tag) { return (v.y == tag) & (v.w == tag); }
#define VM0() __builtin_amdgcn_s_waitcnt(0x0F70)
__device__ __forceinline__ v4u gran(float a, float b, unsigned tag) {
  return v4u{__builtin_bit_cast(unsigned, a), tag, __builtin_bit_cast(unsigned, b), tag};
}
#define OPI(i) ((unsigned)(((i) >> 1) * 128 + ((i) & 1) * 16) * OP_STR)
#define PQ_OFF(i, STR) ((STR) ? (unsigned)(i) * (STR) : OPI(i))
#define POLL4O(G, BASE) POLL4(G, BASE, 0u)
#define PQ_ISSUE(X, BASE, STR) _Pragma("unroll") for (int i_ = 0; i_ < 4; ++i_) X[i_] = LDSC(rs_ws, done_ ? 0xFFFFFF00u : (BASE) + PQ_OFF(i_, STR))
#define PQ_TAKE(G, X) do { \
    asm volatile("" : "+v"(X[0]), "+v"(X[1]), "+v"(X[2]), "+v"(X[3]) :: "memory"); \
    const bool c_ = !done_ & tags_ok(X[0], tag) & tags_ok(X[1], tag) & tags_ok(X[2], tag) & tags_ok(X[3], tag); \
    _Pragma("unroll") for (int i_ = 0; i_ < 4; ++i_) { G[i_].x = c_ ? X[i_].x : G[i_].x; G[i_].z = c_ ? X[i_].z : G[i_].z; } \
    done_ |= c_; } while (0)
#define POLL4(G, BASE, STR) do { \
    bool done_ = tags_ok(G[0], tag) & tags_ok(G[1], tag) & tags_ok(G[2], tag) & tags_ok(G[3], tag); \
    if (__all(done_)) break; \
    v4u X_[4], Y_[4]; \
    asm volatile("" :: "s"(ws) : "memory"); \
    PQ_ISSUE(X_, BASE, STR); \
    __builtin_amdgcn_s_sleep(POLL_HALF); \
    asm volatile("" :: "s"(ws) : "memory"); \
    PQ_ISSUE(Y_, BASE, STR); \
    bool fin_ = false; \
    _Pragma("unroll") for (int r_ = 0; r_ < POLL_ROUNDS; ++r_) { \
      PQ_TAKE(G, X_); \
      if (__all(done_)) { fin_ = true; break; } \
      asm volatile("" :: "s"(ws) : "memory"); \
      PQ_ISSUE(X_, BASE, STR); \
      PQ_TAKE(G, Y_); \
      if (__all(done_)) { fin_ = true; break; } \
      asm volatile("" :: "s"(ws) : "memory"); \
      PQ_ISSUE(Y_, BASE, STR); \
    } \
    if (fin_) break; \
    VM0(); \
    for (unsigned spins_ = 0; spins_ < SPIN_CAP; ++spins_) { \
      PQ_TAKE(G, X_); \
      if (__all(done_)) break; \
      __builtin_amdgcn_s_sleep(2); \
      asm volatile("" :: "s"(ws) : "memory"); \
      PQ_ISSUE(X_, BASE, STR); \
      VM0(); \
    } \
  } while (0)
#define STAMP(i) do { } while (0)
#define FSTAMP(i) do { } while (0)
#define STAMP_FLUSH() do { } while (0)

__device__ __forceinline__ void gemv64(const v4u (&wv)[4], const unsigned* xd  , const unsigned (&mk)[4],
                                       float* red  , int lane) {
  const int g = lane >> 4, bsel = (lane >> 3) & 1;
  f32x4 a0 = {0.f, 0.f, 0.f, 0.f}, a1 = {0.f, 0.f, 0.f, 0.f};
#pragma unroll
  for (int u = 0; u < 4; ++u) {
    const v2u x2 = *(const v2u*)(xd + bsel * 1024 + 8 * u + 2 * g);
    a0 = mfma(v4u{x2.x & mk[0], x2.x & mk[1], x2.x & mk[2], x2.x & mk[3]}, wv[u], a0);
    a1 = mfma(v4u{x2.y & mk[0], x2.y & mk[1], x2.y & mk[2], x2.y & mk[3]}, wv[u], a1);
  }
  f32x4 r;
#pragma unroll
  for (int i = 0; i < 4; ++i) { const float v = bsel ? a1[i] : a0[i]; r[i] = v + dpp<0x128>(v); }
  if (bsel == 0) *(f32x4*)(red + (g >> 1) * 64 + 8 * (lane & 7) + 4 * (g & 1)) = r;
}

__global__ void __launch_bounds__(NTHR, 2)
fused_decode(const u16* __restrict__ x, const u16* __restrict__ kc, const u16* __restrict__ vc, const u16* __restrict__ wq,
             const u16* __restrict__ wo, const u16* __restrict__ w1, const u16* __restrict__ w2, u16* __restrict__ out,
             unsigned char* ws) {
  __shared__ __attribute__((aligned(16))) unsigned char smem[L_END];
  const int tid = threadIdx.x, lane = tid & 63, w = __builtin_amdgcn_readfirstlane(tid >> 6);
  const int j = blockIdx.x;
  const int g = lane >> 4, n16 = lane & 15;
  unsigned dbg_fail = 0; (void)dbg_fail;
  STAMP(0);
  const unsigned tstart = (unsigned)__builtin_amdgcn_s_memrealtime();
  unsigned anchor = tstart;
  const __amdgpu_buffer_rsrc_t rs_ws = mkrs(ws, WS_END);
  if (YPRIO && w >= 4) __builtin_amdgcn_s_setprio(YPRIO);
  const int ks = j & 63, ns = j >> 6;
  const bool has_ffn = ks < 56;
  const unsigned ep = __builtin_amdgcn_raw_buffer_load_b32(rs_ws, WS_E, 0, AUX_SC1);
  const __amdgpu_buffer_rsrc_t rs_x = mkrs(x, 4096);
  v4u xr[2];
  {
    const unsigned xo = (w < 2) ? (unsigned)(w * 2048 + lane * 32) : 0xFFFFFF00u;
    xr[0] = __builtin_amdgcn_raw_buffer_load_b128(rs_x, xo, 0, 0);
    xr[1] = __builtin_amdgcn_raw_buffer_load_b128(rs_x, xo, 16, 0);
  }
  const unsigned xres = __builtin_amdgcn_raw_buffer_load_b32(rs_x, (unsigned)((j >> 7) * 2048 + (j & 127) * 16 + (lane & 3) * 4), 0, 0);
  v4u tq[4], t1[4];
  {
    const __amdgpu_buffer_rsrc_t rs = mkrs(wq, 1024u * 8192u);
    const unsigned vo = (unsigned)(256 * ns + 32 * w + (lane >> 3)) * 8192u + ks * 128 + (lane & 7) * 16;
#pragma unroll
    for (int u = 0; u < 4; ++u) tq[u] = LDNT(rs, vo, u * 8 * 8192);
  }
  {
    const __amdgpu_buffer_rsrc_t rs = mkrs(w1, has_ffn ? 1024u * 7168u : 0u);
    const unsigned vo = (unsigned)(256 * ns + 32 * w + (lane >> 3)) * 7168u + ks * 128 + (lane & 7) * 16;
#pragma unroll
    for (int u = 0; u < 4; ++u) t1[u] = LDNT(rs, vo, u * 8 * 7168);
  }
  const int pair = j >> 5, sp = j & 31, bb = pair >> 2, kvh = pair & 3;
  const bool odd = sp & 1;
  const unsigned char* kbase = (const unsigned char*)kc + (size_t)pair * (8u << 20);
  const unsigned char* vbase = (const unsigned char*)vc + (size_t)pair * (8u << 20);
  const __amdgpu_buffer_rsrc_t rs_k = mkrs(kbase, 8u << 20), rs_v = mkrs(vbase, 8u << 20);
  const __amdgpu_buffer_rsrc_t rs_k2 = mkrs(kbase, odd ? 0u : (8u << 20)), rs_v2 = mkrs(vbase, odd ? 0u : (8u << 20));
  const unsigned wbase = (unsigned)(sp * 8 + w) * PAT_C * 1024u;
  const unsigned kvo = wbase + ((lane >> 2) & 3) * 256 + (4 * g + (lane & 3)) * 16;
  const unsigned vvo = wbase + lane * 16;
  const int st = pair * PAT_STG + (sp * 8 + w) * PAT_ROT, st2 = st + 8 * PAT_ROT;
#define UOFF(n, st_) ((unsigned)((((n) + (st_)) & 31) / PAT_C) * (256u * PAT_C * 1024u) + (unsigned)((((n) + (st_)) & 31) % PAT_C) * 1024u)
#define KVLD(rs, rs2, vo, i) ((i) < 32 - BIAS ? LDNT(rs, vo, UOFF(i, st)) : (i) < 32 ? LDNT(rs2, vo, UOFF(i, st)) : LDNT(rs2, vo, UOFF((i) - BIAS, st2) + 8u * PAT_C * 1024u))
#define ITEM(i) ((i) < NKV ? KVLD(rs_k, rs_k2, kvo, i) : KVLD(rs_v, rs_v2, vvo, (i) - NKV))
#define WAIT_TICK(base, dt) do { unsigned d_; \
    asm volatile("s_branch 2f\n1: s_sleep " PSLEEP "\n2: s_memrealtime vcc\n\ts_waitcnt lgkmcnt(0)\n\ts_sub_u32 %0, vcc_lo, %1\n\ts_cmp_lt_u32 %0, %2\n\ts_cbranch_scc1 1b" \
                 : "=&s"(d_) : "s"(base), "n"(dt) : "vcc", "scc"); } while (0)
#define PACE(i) do { if (TAU16 && (((i) - W) % PACE_EVERY) == 0) { unsigned d_; \
    asm volatile("s_branch 2f\n1: s_sleep " PSLEEP "\n2: s_memrealtime vcc\n\ts_waitcnt lgkmcnt(0)\n\ts_sub_u32 %0, vcc_lo, %1\n\ts_cmp_lt_u32 %0, %2\n\ts_cbranch_scc1 1b" \
                 : "=&s"(d_) : "s"(anchor), "n"(PACE_P0 + ((((i) - W) * TAU16) >> 4)) : "vcc", "scc"); } } while (0)
  v4u a[W];
  SB();
  unsigned mk[4];
  {
    const int jp = lane & 7;
#pragma unroll
    for (int k = 0; k < 4; ++k) mk[k] = ((jp >> 1) == k) ? ((jp & 1) ? 0xffff0000u : 0x0000ffffu) : 0u;
  }
  unsigned mk2[2];
  {
    const int jp = lane & 3;
#pragma unroll
    for (int k = 0; k < 2; ++k) mk2[k] = ((jp >> 1) == k) ? ((jp & 1) ? 0xffff0000u : 0x0000ffffu) : 0u;
  }
  if (w < 2) {
    float v[16];
#pragma unroll
    for (int k = 0; k < 4; ++k) { v[2 * k] = bf2f(xr[0][k]); v[2 * k + 1] = bfhi(xr[0][k]); v[8 + 2 * k] = bf2f(xr[1][k]); v[8 + 2 * k + 1] = bfhi(xr[1][k]); }
    float s = 0.f;
#pragma unroll
    for (int i = 0; i < 16; ++i) s += v[i];
    s = x32_sum(x16_sum(row_sum(s)));
    const float mean = s * (1.f / 1024.f);
    float q = 0.f;
#pragma unroll
    for (int i = 0; i < 16; ++i) { const float d = v[i] - mean; q += d * d; }
    q = x32_sum(x16_sum(row_sum(q)));
    const float var = rbf(q * (1.f / 1024.f));
    const float mb = rbf(mean);
    const float den = rbf(sqrtf(rbf(var + 1e-5f)));
    const float rden = 1.f / den;
    unsigned* xd = (unsigned*)(smem + L_XD) + w * 1024 + lane * 16;
#pragma unroll
    for (int k = 0; k < 4; ++k) {
      v4u o;
#pragma unroll
      for (int i = 0; i < 4; ++i) { const float t = rbf(v[4 * k + i] - mb) * rden; o[i] = cvtpk(t, t); }
      *(v4u*)(xd + 4 * k) = o;
    }
  }
  if (tid == 0) *(unsigned*)(smem + L_MISC + 4) = 0u;
  WG_BARRIER();
  STAMP(1);
  const unsigned tag = __builtin_amdgcn_readfirstlane(ep) + 1u;
  gemv64(tq, (const unsigned*)(smem + L_XD) + 256 * ns + 32 * w, mk, (float*)(smem + L_RQ) + w * 128, lane);
  WG_BARRIER();
  if (w == 0) {
    const float* red = (const float*)(smem + L_RQ) + (lane >> 5) * 64 + 2 * (lane & 31);
    f32x2 s = {0.f, 0.f};
#pragma unroll
    for (int ww = 0; ww < 8; ++ww) s += *(const f32x2*)(red + ww * 128);
    STSC(gran(s.x, s.y, tag), rs_ws, WS_QP + (unsigned)((ns * 2 + (lane >> 5)) * 2048 + ks * 32 + (lane & 31)) * 16u);
    if (lane == 0) STSC((v4u{tstart, tag, tstart, tag}), rs_ws, WS_TS + (unsigned)j * 16u);
  }
  SB();
#pragma unroll
  for (int i = 0; i < 4; ++i) a[i] = ITEM(i);
  SB();
  STAMP(2);
  gemv64(t1, (const unsigned*)(smem + L_XD) + 256 * ns + 32 * w, mk, (float*)(smem + L_RH) + w * 128, lane);
  WG_BARRIER();
  if (w == 1 && has_ffn) {
    const float* red = (const float*)(smem + L_RH) + (lane >> 5) * 64 + 2 * (lane & 31);
    f32x2 s = {0.f, 0.f};
#pragma unroll
    for (int ww = 0; ww < 8; ++ww) s += *(const f32x2*)(red + ww * 128);
    STSC(gran(s.x, s.y, tag), rs_ws, WS_HP + (unsigned)((ns * 2 + (lane >> 5)) * 1792 + ks * 32 + (lane & 31)) * 16u);
  }
  SB();
#pragma unroll
  for (int i = 4; i < EARLY_N; ++i) a[i] = ITEM(i);
  SB();
  STAMP(3);
  {
    const unsigned qo = WS_QP + (unsigned)(bb * 2048 + kvh * 512 + tid) * 16u;
    v4u gq[4];
    WAIT_TICK(tstart, QG_T);
#pragma unroll
    for (int k = 0; k < 4; ++k) gq[k] = LDSC(rs_ws, qo + k * (2 * 2048 * 16));
    const v4u gts = LDSC(rs_ws, (TAU16 && w == 0) ? WS_TS + (unsigned)((lane >> 4) * 64 + kvh * 16 + (lane & 15)) * 16u : 0xFFFFFF00u);
    SB();
#pragma unroll
    for (int i = EARLY_N; i < EARLY_N + 8; ++i) a[i] = ITEM(i);
    SB();
    {
      bool ok = tags_ok(gq[0], tag) & tags_ok(gq[1], tag) & tags_ok(gq[2], tag) & tags_ok(gq[3], tag);
      if (!__all(ok)) {
        unsigned spins = 0;
        do {
          __builtin_amdgcn_s_sleep(2); asm volatile("" :: "s"(ws) : "memory");
          if (!ok) {
#pragma unroll
            for (int k = 0; k < 4; ++k) gq[k] = LDSC(rs_ws, qo + k * (2 * 2048 * 16));
          }
          VM0();
          ok = tags_ok(gq[0], tag) & tags_ok(gq[1], tag) & tags_ok(gq[2], tag) & tags_ok(gq[3], tag);
        } while (!__all(ok) && ++spins < SPIN_CAP);
      }
    }
    if (TAU16 && w == 0) {
      int dt = tags_ok(gts, tag) ? (int)(gts.x - tstart) : 0;
      dt = min(dt, 0);
      dt = min(dt, __builtin_amdgcn_mov_dpp(dt, 0xB1, 0xf, 0xf, true)); dt = min(dt, __builtin_amdgcn_mov_dpp(dt, 0x4E, 0xf, 0xf, true));
      dt = min(dt, __builtin_amdgcn_mov_dpp(dt, 0x141, 0xf, 0xf, true)); dt = min(dt, __builtin_amdgcn_mov_dpp(dt, 0x140, 0xf, 0xf, true));
      const int d0 = __builtin_amdgcn_readlane(dt, 0), d1 = __builtin_amdgcn_readlane(dt, 16), d2 = __builtin_amdgcn_readlane(dt, 32), d3 = __builtin_amdgcn_readlane(dt, 48);
      if (lane == 0) *(unsigned*)(smem + L_MISC) = tstart + (unsigned)max(min(min(d0, d1), min(d2, d3)), -300);
    }
    const float q0 = (__uint_as_float(gq[0].x) + __uint_as_float(gq[1].x)) + (__uint_as_float(gq[2].x) + __uint_as_float(gq[3].x));
    const float q1 = (__uint_as_float(gq[0].z) + __uint_as_float(gq[1].z)) + (__uint_as_float(gq[2].z) + __uint_as_float(gq[3].z));
    ((unsigned*)(smem + L_QS))[tid] = cvtpk(q0, q1);
  }
  WG_BARRIER();
  STAMP(4);
  if (TAU16) anchor = __builtin_amdgcn_readfirstlane(*(const unsigned*)(smem + L_MISC));
#pragma unroll
  for (int i = EARLY_N + 8; i < W; ++i) a[i] = ITEM(i);
  SB();
  const int headn = 4 * (n16 & 1) + (n16 >> 2), bpp = (n16 >> 1) & 1;
  v4u qb[2];
#pragma unroll
  for (int t = 0; t < 2; ++t) qb[t] = *(const v4u*)(smem + L_QS + headn * 256 + (4 * g + 2 * t + bpp) * 16);
  const unsigned hobase = WS_HP + (unsigned)((lane >> 5) * 1792 + ks * 32 + (lane & 31)) * 16u;
  const bool hact = (w == 2 && has_ffn);
  v4u gh[4];
#pragma unroll
  for (int k = 0; k < 4; ++k) gh[k] = LDSC(rs_ws, hact ? hobase + k * (2 * 1792 * 16) : 0xFFFFFF00u);
  SB();
  float S[NKV];
#pragma unroll
  for (int i = 0; i < NKV; ++i) {
    const v4u cur = a[i % W];
    if (i + RD >= W) { PACE(i + RD); a[(i + RD) % W] = ITEM(i + RD); }
    const f32x4 z = {0.f, 0.f, 0.f, 0.f};
    const f32x4 d0 = mfma(cur, qb[0], z), d1 = mfma(cur, qb[1], z);
    const float t1_ = d0[0] + d1[2], t2_ = d0[1] + d1[3];
    const float x_ = bpp ? t2_ : t1_;
    S[i] = x_ + dpp<0x4E>(x_);
    if (i >= 32 - BIAS && odd) S[i] = -INFINITY;
    if (FINE && ((i) & 3) == 3) asm volatile("" :: "v"(S[i]));
    FSTAMP(i);
    SB();
  }
  STAMP(5);
  float mr;
  {
    float mx = S[0];
#pragma unroll
    for (int i = 1; i < NKV; ++i) mx = fmaxf(mx, S[i]);
    mx = x32_max(x16_max(mx));
    mr = rbf(rbf(mx) * SCALE);
  }
  v4u tw[8];
  const __amdgpu_buffer_rsrc_t rs_w2 = mkrs(w2, has_ffn ? 3584u * 2048u : 0u);
  const __amdgpu_buffer_rsrc_t rs_wo = mkrs(wo, 4096u * 2048u);
  const unsigned wvo = (unsigned)(64 * ks + 8 * w + g) * 2048u + ns * 512 + n16 * 16;
#define WLD(k) ((k) < 4 ? LDNT(rs_w2, wvo, (((k) >> 1) & 1) * 4 * 2048 + ((k) & 1) * 256) : LDNT(rs_wo, wvo, (((k) >> 1) & 1) * 4 * 2048 + ((k) & 1) * 256))
  float lsum = 0.f;
  const float mc = mr * LOG2E;
  unsigned P[NKV];
#pragma unroll
  for (int i = 0; i < NKV; ++i) {
    if ((i & 3) == 0 && (i >> 2) < TOPUP) { PACE(NKV + RD + (i >> 2)); a[(NKV + RD + (i >> 2)) % W] = ITEM(NKV + RD + (i >> 2)); SB(); }
    const float sr = rbf(rbf(S[i]) * SCALE);
    const float p = __builtin_amdgcn_exp2f(sr * LOG2E - mc);
    lsum += p;
    P[i] = cvtpk(p, p);
  }
  lsum = x32_sum(x16_sum(lsum));
  if (g == 0 && bpp == 0) { ((float*)(smem + L_MX))[headn * 8 + w] = mr; ((float*)(smem + L_LS))[headn * 8 + w] = lsum; }
  asm volatile("s_waitcnt lgkmcnt(0)" ::: "memory");
  if (lane == 0) __hip_atomic_fetch_add((LAS unsigned*)(smem + L_MISC + 4), 1u, __ATOMIC_RELAXED, __HIP_MEMORY_SCOPE_WORKGROUP);
  SB();
  f32x4 acc[4];
#pragma unroll
  for (int t = 0; t < 4; ++t) acc[t] = f32x4{0.f, 0.f, 0.f, 0.f};
  v4u gml[4];
  const unsigned mlo = WS_ML + (unsigned)((pair * 32 + (lane & 31)) * 8 + (lane >> 5)) * 16u;
#define VSTEPS(lo, hi) _Pragma("unroll") for (int i = NKV + (lo); i < NKV + (hi); ++i) { \
    const v4u cur = a[i % W]; \
    if (i + RD < 2 * NKV && i + RD >= NKV + RD + TOPUP) { PACE(i + RD); a[(i + RD) % W] = ITEM(i + RD); } \
    if (i >= 2 * NKV - W_IN_LOOP) { if (TAU16 && i == 2 * NKV - W_IN_LOOP) WAIT_TICK(anchor, PACE_P0 + (((2 * NKV - W) * TAU16) >> 4)); tw[i - (2 * NKV - W_IN_LOOP)] = WLD(i - (2 * NKV - W_IN_LOOP)); } \
    if (ABL & 2) { acc[0][0] += __uint_as_float((cur.x ^ cur.y ^ cur.z ^ cur.w ^ P[i - NKV]) & 0x3fffffffu); SB(); continue; } \
    const unsigned pp = P[i - NKV]; \
    const unsigned p0 = dppu<0x00>(pp), p1 = dppu<0x55>(pp); \
    acc[0] = mfma16(p0 & mk2[0], p0 & mk2[1], cur.x, cur.y, acc[0]); \
    acc[1] = mfma16(p0 & mk2[0], p0 & mk2[1], cur.z, cur.w, acc[1]); \
    acc[2] = mfma16(p1 & mk2[0], p1 & mk2[1], cur.x, cur.y, acc[2]); \
    acc[3] = mfma16(p1 & mk2[0], p1 & mk2[1], cur.z, cur.w, acc[3]); \
    if (FINE && ((i) & 3) == 3) asm volatile("" :: "v"(acc[3])); \
    FSTAMP(i); \
    SB(); \
  }
  static_assert(ML_PUB_STEP <= H_USE_STEP && H_USE_STEP <= ML_GET_STEP && ML_GET_STEP <= ML_USE_STEP && ML_USE_STEP <= 32 - BIAS, "order of the side jobs");
  VSTEPS(0, ML_PUB_STEP)
    WG_BARRIER();
    if (w == 7) {
      WAIT_WAVES();
      if (g == 0 && bpp == 0) {
        const f32x4 m0 = *(const f32x4*)(smem + L_MX + headn * 32), m1 = *(const f32x4*)(smem + L_MX + headn * 32 + 16);
        const f32x4 l0 = *(const f32x4*)(smem + L_LS + headn * 32), l1 = *(const f32x4*)(smem + L_LS + headn * 32 + 16);
        const float M = fmaxf(fmaxf(fmaxf(m0[0], m0[1]), fmaxf(m0[2], m0[3])), fmaxf(fmaxf(m1[0], m1[1]), fmaxf(m1[2], m1[3])));
        float L = 0.f;
#pragma unroll
        for (int k = 0; k < 4; ++k) L += __builtin_amdgcn_exp2f((m0[k] - M) * LOG2E) * l0[k] + __builtin_amdgcn_exp2f((m1[k] - M) * LOG2E) * l1[k];
        STSC(gran(M, L, tag), rs_ws, WS_ML + (unsigned)((pair * 32 + sp) * 8 + headn) * 16u);
      }
    }
  SB();
  VSTEPS(ML_PUB_STEP, H_USE_STEP)
    if (w == 2) {
      bool ok = !has_ffn | (tags_ok(gh[0], tag) & tags_ok(gh[1], tag) & tags_ok(gh[2], tag) & tags_ok(gh[3], tag));
      if (!__all(ok)) {
        unsigned spins = 0;
        do {
          __builtin_amdgcn_s_sleep(2); asm volatile("" :: "s"(ws) : "memory");
#pragma unroll
          for (int k = 0; k < 4; ++k) gh[k] = LDSC(rs_ws, hobase + k * (2 * 1792 * 16));
          VM0();
          ok = !has_ffn | (tags_ok(gh[0], tag) & tags_ok(gh[1], tag) & tags_ok(gh[2], tag) & tags_ok(gh[3], tag));
        } while (!__all(ok) && ++spins < SPIN_CAP);
      }
      const float h0 = rbf((__uint_as_float(gh[0].x) + __uint_as_float(gh[1].x)) + (__uint_as_float(gh[2].x) + __uint_as_float(gh[3].x)));
      const float h1 = rbf((__uint_as_float(gh[0].z) + __uint_as_float(gh[1].z)) + (__uint_as_float(gh[2].z) + __uint_as_float(gh[3].z)));
      const float s0 = h0 / (1.f + __expf(-h0)), s1 = h1 / (1.f + __expf(-h1));
      v2u o; o.x = cvtpk(s0, s0); o.y = cvtpk(s1, s1);
      if (!has_ffn) { o.x = 0u; o.y = 0u; }
      *(v2u*)(smem + L_HD + ((lane >> 5) * 64 + 2 * (lane & 31)) * 4) = o;
    }
  SB();
  VSTEPS(H_USE_STEP, ML_GET_STEP)
    {
#pragma unroll
      for (int k = 0; k < 4; ++k) gml[k] = LDSC(rs_ws, w == 5 ? mlo + k * 32 : 0xFFFFFF00u);
    }
  SB();
  VSTEPS(ML_GET_STEP, ML_USE_STEP)
    if (w == 5) {
      bool ok = tags_ok(gml[0], tag) & tags_ok(gml[1], tag) & tags_ok(gml[2], tag) & tags_ok(gml[3], tag);
      if (!__all(ok)) {
        unsigned spins = 0;
        do {
          __builtin_amdgcn_s_sleep(2); asm volatile("" :: "s"(ws) : "memory");
#pragma unroll
          for (int k = 0; k < 4; ++k) gml[k] = LDSC(rs_ws, mlo + k * 32);
          VM0();
          ok = tags_ok(gml[0], tag) & tags_ok(gml[1], tag) & tags_ok(gml[2], tag) & tags_ok(gml[3], tag);
        } while (!__all(ok) && ++spins < SPIN_CAP);
      }
      WAIT_WAVES();
#pragma unroll
      for (int k = 0; k < 4; ++k) {
        const float m = __uint_as_float(gml[k].x), l = __uint_as_float(gml[k].z);
        const float M = x16_max(row_max(m));
        const float L = x16_sum(row_sum(__builtin_amdgcn_exp2f((m - M) * LOG2E) * l));
        const int e = (2 * k + (lane >> 5)) * 8 + (lane & 7);
        if ((lane & 31) < 8) ((float*)(smem + L_FS))[e] = __builtin_amdgcn_exp2f((((const float*)(smem + L_MX))[e] - M) * LOG2E) / L;
      }
    }
  SB();
  VSTEPS(ML_USE_STEP, NKV)
  STAMP(7);
#pragma unroll
  for (int t = 0; t < 4; ++t) *(f32x4*)(smem + L_MRG + ((w * 4 + t) * 64 + lane) * 16) = acc[t];
  WG_BARRIER();
  STAMP(8);
  {
    const int hl = w, d = 2 * lane;
    const unsigned char* src = smem + L_MRG + ((2 * (hl >> 2) + ((d >> 2) & 1)) * 64 + (hl & 3) * 16 + (d >> 3)) * 16 + (d & 3) * 4;
    const f32x4 c0 = *(const f32x4*)(smem + L_FS + hl * 32), c1 = *(const f32x4*)(smem + L_FS + hl * 32 + 16);
    f32x2 s = {0.f, 0.f};
#pragma unroll
    for (int ww = 0; ww < 8; ++ww) s += (ww < 4 ? c0[ww & 3] : c1[ww & 3]) * *(const f32x2*)(src + ww * 4096);
    STSC(gran(s.x, s.y, tag), rs_ws, WS_OP + (unsigned)j * OP_STR + (unsigned)tid * 16u);
  }
  SB();
  STAMP(9);
  const unsigned tpub = (unsigned)__builtin_amdgcn_s_memrealtime();
#pragma unroll
  for (int k = W_IN_LOOP; k < 8; ++k) tw[k] = WLD(k);
  SB();
  STAMP(10);
  const unsigned opo = WS_OP + (unsigned)(((ks >> 4) * 32 + (lane >> 2)) * OP_STR) + (unsigned)(((ks >> 1) & 7) * 1024 + (ks & 1) * 512 + w * 64 + (lane & 3) * 16);
  v4u go[4];
  WAIT_TICK(tpub, OG_DELAY);
  STAMP(12);
#pragma unroll
  for (int i = 0; i < 4; ++i) go[i] = LDSC(rs_ws, opo + OPI(i));
  SB();
  f32x4 facc[2];
  {
    const unsigned* hd = (const unsigned*)(smem + L_HD) + ((lane >> 3) & 1) * 64 + 8 * w + g;
    const unsigned h0 = hd[0], h1 = hd[4];
    const v4u A0 = {h0 & mk[0], h0 & mk[1], h0 & mk[2], h0 & mk[3]}, A1 = {h1 & mk[0], h1 & mk[1], h1 & mk[2], h1 & mk[3]};
    facc[0] = mfma(A1, tw[2], mfma(A0, tw[0], f32x4{0.f, 0.f, 0.f, 0.f}));
    facc[1] = mfma(A1, tw[3], mfma(A0, tw[1], f32x4{0.f, 0.f, 0.f, 0.f}));
  }
  asm volatile("" :: "v"(facc[0]), "v"(facc[1]));
  SB();
  STAMP(11);
  {
    POLL4O(go, opo);
    STAMP(13);
    float ov[2][2];
#pragma unroll
    for (int b = 0; b < 2; ++b) {
      float o0 = __uint_as_float(go[2 * b].x) + __uint_as_float(go[2 * b + 1].x);
      float o1 = __uint_as_float(go[2 * b].z) + __uint_as_float(go[2 * b + 1].z);
      o0 += dpp<0x124>(o0); o0 += dpp<0x128>(o0); o0 = x32_sum(x16_sum(o0));
      o1 += dpp<0x124>(o1); o1 += dpp<0x128>(o1); o1 = x32_sum(x16_sum(o1));
      ov[b][0] = o0; ov[b][1] = o1;
    }
    const bool bsel = (lane >> 3) & 1, csel = g & 1;
    const float v0 = csel ? ov[0][1] : ov[0][0], v1 = csel ? ov[1][1] : ov[1][0];
    const float vs = bsel ? v1 : v0;
    const unsigned pk = cvtpk(vs, vs);
    unsigned od0 = __builtin_amdgcn_update_dpp(0, (int)pk, 0x00, 0x3, 0xf, false);
    od0 = __builtin_amdgcn_update_dpp((int)od0, (int)pk, 0x55, 0xC, 0xf, false);
    unsigned od1 = __builtin_amdgcn_update_dpp(0, (int)pk, 0xAA, 0x3, 0xf, false);
    od1 = __builtin_amdgcn_update_dpp((int)od1, (int)pk, 0xFF, 0xC, 0xf, false);
    const v4u A0 = {od0 & mk[0], od0 & mk[1], od0 & mk[2], od0 & mk[3]}, A1 = {od1 & mk[0], od1 & mk[1], od1 & mk[2], od1 & mk[3]};
    const f32x4 r0 = mfma(A1, tw[6], mfma(A0, tw[4], facc[0]));
    const f32x4 r1 = mfma(A1, tw[7], mfma(A0, tw[5], facc[1]));
    *(f32x4*)(smem + L_FIN + ((w * 2 + 0) * 64 + lane) * 16) = r0;
    *(f32x4*)(smem + L_FIN + ((w * 2 + 1) * 64 + lane) * 16) = r1;
  }
  WG_BARRIER();
  if (tid < 256) {
    const int b = tid >> 7, cp = tid & 127, cc = 2 * cp;
    const int cb = cc >> 7, c = (cc & 127) >> 3, half = (cc >> 2) & 1, i0 = cc & 3;
    const unsigned char* src = smem + L_FIN + (cb * 64 + (2 * b + half) * 16 + c) * 16 + i0 * 4;
    f32x2 s = {0.f, 0.f};
#pragma unroll
    for (int ww = 0; ww < 8; ++ww) s += *(const f32x2*)(src + ww * 2048);
    STSC(gran(s.x, s.y, tag), rs_ws, WS_FP + (unsigned)j * FP_STR + (unsigned)tid * 16u);
  }
  STAMP(14);
  if (w == 7) {
    const unsigned fo = WS_FP + (unsigned)((((j >> 5) & 3) * 64 + (lane >> 2)) * FP_STR) + (unsigned)((j >> 7) * 2048 + (j & 31) * 64 + (lane & 3) * 16);
    { const unsigned tf = (unsigned)__builtin_amdgcn_s_memrealtime(); WAIT_TICK(tf, FG_DELAY); }
    v4u gf[4];
#pragma unroll
    for (int u = 0; u < 4; ++u) gf[u] = LDSC(rs_ws, fo + u * (16 * FP_STR));
    POLL4(gf, fo, 16 * FP_STR);
    float s0 = 0.f, s1 = 0.f;
#pragma unroll
    for (int u = 0; u < 4; ++u) { s0 += __uint_as_float(gf[u].x); s1 += __uint_as_float(gf[u].z); }
    s0 += dpp<0x124>(s0); s0 += dpp<0x128>(s0); s0 = x32_sum(x16_sum(s0));
    s1 += dpp<0x124>(s1); s1 += dpp<0x128>(s1); s1 = x32_sum(x16_sum(s1));
    if (lane < 4) ((unsigned*)out)[(j >> 7) * 512 + (j & 127) * 4 + lane] = cvtpk(bf2f(xres) + s0, bfhi(xres) + s1);
    if (j == 0 && lane == 0) __builtin_amdgcn_raw_buffer_store_b32(tag, rs_ws, WS_E, 0, AUX_SC1);
    STAMP(15);
  }
  STAMP_FLUSH();
}

extern "C" void kernel_launch(void* const* d_in, const int* in_sizes, int n_in, void* d_out, int out_size, void* d_ws, size_t ws_size,
                              hipStream_t stream) {
  fused_decode<<<NWG, NTHR, 0, stream>>>((const u16*)d_in[0], (const u16*)d_in[1], (const u16*)d_in[2], (const u16*)d_in[3],
                                         (const u16*)d_in[4], (const u16*)d_in[6], (const u16*)d_in[7], (u16*)d_out,
                                         (unsigned char*)d_ws);
}
```

```cpp
#include <hip/hip_runtime.h>
#include <stdint.h>

typedef unsigned v4u __attribute__((ext_vector_type(4)));
typedef unsigned v2u __attribute__((ext_vector_type(2)));
typedef float f32x4 __attribute__((ext_vector_type(4)));
typedef float f32x2 __attribute__((ext_vector_type(2)));
typedef short bf16x8 __attribute__((ext_vector_type(8)));
typedef short s16x4 __attribute__((ext_vector_type(4)));
typedef __bf16 bf16x2 __attribute__((ext_vector_type(2)));
typedef unsigned short u16;

#define PAT_C 4
#define PAT_STG 4
#define PAT_ROT 1
#define RING_W 20
#define RING_D 8
#define EARLY_N 8
#define QG_T 500
#define W_IN_LOOP 3
#define OG_DELAY 120
#define FG_DELAY 70
#define POLL_HALF 10
#define POLL_ROUNDS 4
#define ML_PUB_STEP 4
#define H_USE_STEP 8
#define ML_GET_STEP 14
#define ML_USE_STEP 26
#define EARLY_ARGS 1
#define TAU16 0
#define PACE_P0 840
#define PAD_B 256

constexpr int NWG = 256, NTHR = 512, NKV = 32;
constexpr int W = RING_W, RD = RING_D;
constexpr int TOPUP = (W - RD) < 8 ? (W - RD) : 8;
constexpr unsigned OP_STR = 4096 + PAD_B, FP_STR = 2048 + PAD_B;
constexpr unsigned WS_E = 0;
constexpr unsigned WS_QP = 4096;
constexpr unsigned WS_HP = WS_QP + 4 * 2 * 1024 * 16;
constexpr unsigned WS_ML = WS_HP + 4 * 2 * 896 * 16;
constexpr unsigned WS_OP = WS_ML + 8 * 32 * 8 * 16;
constexpr unsigned WS_FP = WS_OP + 256 * OP_STR;
constexpr unsigned WS_END = WS_FP + 256 * FP_STR;
constexpr unsigned WS_STAMP = WS_END;
constexpr int L_XD = 0, L_RQ = 8192, L_RH = 12288, L_QS = 16384, L_HD = 18432, L_MX = 18944, L_LS = 19200, L_FS = 19456, L_MISC = 19712,
              L_MRG = 19776, L_FIN = L_MRG + 32768, L_END = L_FIN + 32768;
constexpr unsigned SPIN_CAP = 1u << 16;
constexpr int AUX_NT = 2, AUX_SC1 = 16;
constexpr unsigned OOB = 0xFFFFFF00u;
constexpr float SCALE = 0.08838834764831845f, LOG2E = 1.4426950408889634f;

__device__ __forceinline__ float bf2f(unsigned b) { return __builtin_bit_cast(float, b << 16); }
__device__ __forceinline__ float bfhi(unsigned b) { return __builtin_bit_cast(float, b & 0xffff0000u); }
__device__ __forceinline__ unsigned cvtpk(float lo, float hi) {
  return __builtin_bit_cast(unsigned, __builtin_convertvector(f32x2{lo, hi}, bf16x2));
}
__device__ __forceinline__ float rbf(float x) { return bfhi(cvtpk(x, x)); }
__device__ __forceinline__ f32x4 mfma(v4u a, v4u b, f32x4 c) {
  return __builtin_amdgcn_mfma_f32_16x16x32_bf16(__builtin_bit_cast(bf16x8, a), __builtin_bit_cast(bf16x8, b), c, 0, 0, 0);
}
__device__ __forceinline__ f32x4 mfma16(unsigned a0, unsigned a1, unsigned b0, unsigned b1, f32x4 c) {
  return __builtin_amdgcn_mfma_f32_16x16x16bf16_1k(__builtin_bit_cast(s16x4, v2u{a0, a1}), __builtin_bit_cast(s16x4, v2u{b0, b1}), c, 0, 0, 0);
}
template <int CTRL> __device__ __forceinline__ float dpp(float x) {
  return __builtin_bit_cast(float, __builtin_amdgcn_mov_dpp(__builtin_bit_cast(int, x), CTRL, 0xf, 0xf, true));
}
template <int CTRL> __device__ __forceinline__ unsigned dppu(unsigned x) { return (unsigned)__builtin_amdgcn_mov_dpp((int)x, CTRL, 0xf, 0xf, true); }
__device__ __forceinline__ float x16_max(float x) {
  auto s = __builtin_amdgcn_permlane16_swap(__float_as_uint(x), __float_as_uint(x), false, false);
  return fmaxf(__uint_as_float(s[0]), __uint_as_float(s[1]));
}
__device__ __forceinline__ float x16_sum(float x) {
  auto s = __builtin_amdgcn_permlane16_swap(__float_as_uint(x), __float_as_uint(x), false, false);
  return __uint_as_float(s[0]) + __uint_as_float(s[1]);
}
__device__ __forceinline__ float x32_max(float x) {
  auto s = __builtin_amdgcn_permlane32_swap(__float_as_uint(x), __float_as_uint(x), false, false);
  return fmaxf(__uint_as_float(s[0]), __uint_as_float(s[1]));
}
__device__ __forceinline__ float x32_sum(float x) {
  auto s = __builtin_amdgcn_permlane32_swap(__float_as_uint(x), __float_as_uint(x), false, false);
  return __uint_as_float(s[0]) + __uint_as_float(s[1]);
}
__device__ __forceinline__ float row_sum(float x) {
  x += dpp<0xB1>(x); x += dpp<0x4E>(x); x += dpp<0x141>(x); x += dpp<0x140>(x); return x;
}
__device__ __forceinline__ float row_max(float x) {
  x = fmaxf(x, dpp<0xB1>(x)); x = fmaxf(x, dpp<0x4E>(x)); x = fmaxf(x, dpp<0x141>(x)); x = fmaxf(x, dpp<0x140>(x)); return x;
}
__device__ __forceinline__ float par_sum(float x) {
  x += dpp<0x4E>(x); x += dpp<0x124>(x); x += dpp<0x128>(x); return x32_sum(x16_sum(x));
}
#define LAS __attribute__((address_space(3)))
#define WG_BARRIER() do { asm volatile("s_waitcnt lgkmcnt(0)" ::: "memory"); __builtin_amdgcn_s_barrier(); asm volatile("" ::: "memory"); } while (0)
#define SB() __builtin_amdgcn_sched_barrier(0)
#define VM0() __builtin_amdgcn_s_waitcnt(0x0F70)
#define MEMFENCE() asm volatile("" :: "s"(ws) : "memory")
#define LDNT(rs, vo, so) __builtin_amdgcn_raw_buffer_load_b128(rs, vo, so, AUX_NT)
#define LDSC(rs, vo) __builtin_amdgcn_raw_buffer_load_b128(rs, vo, 0, AUX_SC1)
#define STSC(v, rs, vo) __builtin_amdgcn_raw_buffer_store_b128(v, rs, vo, 0, AUX_SC1)
#define WAIT_TICK(base, dt) do { unsigned d_; \
    asm volatile("s_branch 2f\n1: s_sleep 1\n2: s_memrealtime vcc\n\ts_waitcnt lgkmcnt(0)\n\ts_sub_u32 %0, vcc_lo, %1\n\ts_cmp_lt_u32 %0, %2\n\ts_cbranch_scc1 1b" \
                 : "=&s"(d_) : "s"(base), "n"(dt) : "vcc", "scc"); } while (0)
#define WAIT_WAVES() do { unsigned n_ = 0; while (__hip_atomic_load((LAS unsigned*)(smem + L_MISC), __ATOMIC_RELAXED, __HIP_MEMORY_SCOPE_WORKGROUP) < 8u && ++n_ < SPIN_CAP) __builtin_amdgcn_s_sleep(1); asm volatile("" ::: "memory"); } while (0)
__device__ __forceinline__ __amdgpu_buffer_rsrc_t mkrs(const void* p, unsigned bytes) {
  return __builtin_amdgcn_make_buffer_rsrc((void*)p, 0, (int)bytes, 0x00020000);
}
__device__ __forceinline__ bool tags_ok(v4u v, unsigned tag) { return (v.y == tag) & (v.w == tag); }
__device__ __forceinline__ v4u gran2(float a, float b, unsigned tag) {
  return v4u{__builtin_bit_cast(unsigned, a), tag, __builtin_bit_cast(unsigned, b), tag};
}
__device__ __forceinline__ v4u gran4(f32x4 v, unsigned tag) {
  v4u r;
#pragma unroll
  for (int k = 0; k < 4; ++k) r[k] = ((__float_as_uint(v[k]) + 0x80u) & 0xFFFFFF00u) | ((tag >> (8 * k)) & 0xFFu);
  return r;
}
__device__ __forceinline__ bool g4_ok(v4u v, unsigned tag) {
  const unsigned lo = __builtin_amdgcn_perm(v.y, v.x, 0x0c0c0400u), hi = __builtin_amdgcn_perm(v.w, v.z, 0x04000c0cu);
  return (lo | hi) == tag;
}
__device__ __forceinline__ f32x4 g4_val(v4u v) {
  return f32x4{__uint_as_float(v.x & 0xFFFFFF00u), __uint_as_float(v.y & 0xFFFFFF00u), __uint_as_float(v.z & 0xFFFFFF00u), __uint_as_float(v.w & 0xFFFFFF00u)};
}
#define PQ_ISSUE(X, BASE, STR) do { X[0] = LDSC(rs_ws, done_ ? OOB : (BASE)); X[1] = LDSC(rs_ws, done_ ? OOB : (BASE) + (STR)); } while (0)
#define PQ_TAKE(G, X) do { \
    asm volatile("" : "+v"(X[0]), "+v"(X[1]) :: "memory"); \
    const bool c_ = !done_ & g4_ok(X[0], tag) & g4_ok(X[1], tag); \
    _Pragma("unroll") for (int k_ = 0; k_ < 4; ++k_) { G[0][k_] = c_ ? X[0][k_] : G[0][k_]; G[1][k_] = c_ ? X[1][k_] : G[1][k_]; } \
    done_ |= c_; } while (0)
#define POLL2(G, BASE, STR) do { \
    bool done_ = g4_ok(G[0], tag) & g4_ok(G[1], tag); \
    if (__all(done_)) break; \
    v4u X_[2], Y_[2]; \
    MEMFENCE(); PQ_ISSUE(X_, BASE, STR); \
    __builtin_amdgcn_s_sleep(POLL_HALF); \
    MEMFENCE(); PQ_ISSUE(Y_, BASE, STR); \
    bool fin_ = false; \
    _Pragma("unroll") for (int r_ = 0; r_ < POLL_ROUNDS; ++r_) { \
      PQ_TAKE(G, X_); \
      if (__all(done_)) { fin_ = true; break; } \
      MEMFENCE(); PQ_ISSUE(X_, BASE, STR); \
      PQ_TAKE(G, Y_); \
      if (__all(done_)) { fin_ = true; break; } \
      MEMFENCE(); PQ_ISSUE(Y_, BASE, STR); \
    } \
    if (fin_) break; \
    VM0(); \
    for (unsigned spins_ = 0; spins_ < SPIN_CAP; ++spins_) { \
      PQ_TAKE(G, X_); \
      if (__all(done_)) break; \
      __builtin_amdgcn_s_sleep(2); \
      MEMFENCE(); PQ_ISSUE(X_, BASE, STR); \
      VM0(); \
    } \
  } while (0)
#define STAMP(i) do { } while (0)
#define STAMP_FLUSH() do { } while (0)

__device__ __forceinline__ void gemv64(const v4u (&wv)[4], const unsigned* xd  , const unsigned (&mk)[4],
                                       float* red  , int lane) {
  const int g = lane >> 4, bsel = (lane >> 3) & 1;
  f32x4 a0 = {0.f, 0.f, 0.f, 0.f}, a1 = {0.f, 0.f, 0.f, 0.f};
#pragma unroll
  for (int u = 0; u < 4; ++u) {
    const v2u x2 = *(const v2u*)(xd + bsel * 1024 + 8 * u + 2 * g);
    a0 = mfma(v4u{x2.x & mk[0], x2.x & mk[1], x2.x & mk[2], x2.x & mk[3]}, wv[u], a0);
    a1 = mfma(v4u{x2.y & mk[0], x2.y & mk[1], x2.y & mk[2], x2.y & mk[3]}, wv[u], a1);
  }
  f32x4 r;
#pragma unroll
  for (int i = 0; i < 4; ++i) { const float v = bsel ? a1[i] : a0[i]; r[i] = v + dpp<0x128>(v); }
  if (bsel == 0) *(f32x4*)(red + (g >> 1) * 64 + 8 * (lane & 7) + 4 * (g & 1)) = r;
}

__global__ void __launch_bounds__(NTHR, 2)
fused_decode(const u16* __restrict__ x, const u16* __restrict__ kc, const u16* __restrict__ vc, const u16* __restrict__ wq,
             const u16* __restrict__ wo, const u16* __restrict__ w1, const u16* __restrict__ w2, u16* __restrict__ out,
             unsigned char* ws) {
  __shared__ __attribute__((aligned(16))) unsigned char smem[L_END];
  const int tid = threadIdx.x, lane = tid & 63, w = __builtin_amdgcn_readfirstlane(tid >> 6);
  const int j = blockIdx.x;
  const int g = lane >> 4, n16 = lane & 15;
  if (EARLY_ARGS) asm volatile("" :: "s"(x), "s"(kc), "s"(vc), "s"(wq), "s"(wo), "s"(w1), "s"(w2), "s"(out), "s"(ws));
  STAMP(0);
  const unsigned tstart = (unsigned)__builtin_amdgcn_s_memrealtime();
  const __amdgpu_buffer_rsrc_t rs_ws = mkrs(ws, WS_END);
  const int ks = j & 63, ns = j >> 6;
  const bool has_ffn = ks < 56;
  const unsigned ep = __builtin_amdgcn_raw_buffer_load_b32(rs_ws, WS_E, 0, AUX_SC1);
  const __amdgpu_buffer_rsrc_t rs_x = mkrs(x, 4096);
  v4u xr[2];
  {
    const unsigned xo = (w < 2) ? (unsigned)(w * 2048 + lane * 32) : OOB;
    xr[0] = __builtin_amdgcn_raw_buffer_load_b128(rs_x, xo, 0, 0);
    xr[1] = __builtin_amdgcn_raw_buffer_load_b128(rs_x, xo, 16, 0);
  }
  const v2u xres = __builtin_amdgcn_raw_buffer_load_b64(rs_x, (unsigned)((j >> 7) * 2048 + (j & 127) * 16 + (lane & 1) * 8), 0, 0);
  v4u tq[4], t1[4];
  {
    const __amdgpu_buffer_rsrc_t rs = mkrs(wq, 1024u * 8192u);
    const unsigned vo = (unsigned)(256 * ns + 32 * w + (lane >> 3)) * 8192u + ks * 128 + (lane & 7) * 16;
#pragma unroll
    for (int u = 0; u < 4; ++u) tq[u] = LDNT(rs, vo, u * 8 * 8192);
  }
  {
    const __amdgpu_buffer_rsrc_t rs = mkrs(w1, has_ffn ? 1024u * 7168u : 0u);
    const unsigned vo = (unsigned)(256 * ns + 32 * w + (lane >> 3)) * 7168u + ks * 128 + (lane & 7) * 16;
#pragma unroll
    for (int u = 0; u < 4; ++u) t1[u] = LDNT(rs, vo, u * 8 * 7168);
  }
  const int pair = j >> 5, sp = j & 31, bb = pair >> 2, kvh = pair & 3;
  const __amdgpu_buffer_rsrc_t rs_k = mkrs((const unsigned char*)kc + (size_t)pair * (8u << 20), 8u << 20);
  const __amdgpu_buffer_rsrc_t rs_v = mkrs((const unsigned char*)vc + (size_t)pair * (8u << 20), 8u << 20);
  const unsigned wbase = (unsigned)(sp * 8 + w) * PAT_C * 1024u;
  const unsigned kvo = wbase + ((lane >> 2) & 3) * 256 + (4 * g + (lane & 3)) * 16;
  const unsigned vvo = wbase + lane * 16;
  const int st = pair * PAT_STG + (sp * 8 + w) * PAT_ROT;
#define UOFF(n) ((unsigned)((((n) + st) & 31) / PAT_C) * (256u * PAT_C * 1024u) + (unsigned)((((n) + st) & 31) % PAT_C) * 1024u)
#define PACE(i) do { if (TAU16 && (((i) - W) & 3) == 0) WAIT_TICK(tstart, PACE_P0 + ((((i) - W) * TAU16) >> 4)); } while (0)
#define ITEM(i) ((i) < NKV ? LDNT(rs_k, kvo, UOFF(i)) : LDNT(rs_v, vvo, UOFF((i) - NKV)))
  v4u a[W];
  SB();
  unsigned mk[4], mk2[2];
  {
    const int jp = lane & 7;
#pragma unroll
    for (int k = 0; k < 4; ++k) mk[k] = ((jp >> 1) == k) ? ((jp & 1) ? 0xffff0000u : 0x0000ffffu) : 0u;
#pragma unroll
    for (int k = 0; k < 2; ++k) mk2[k] = (((jp & 3) >> 1) == k) ? ((jp & 1) ? 0xffff0000u : 0x0000ffffu) : 0u;
  }
  if (w < 2) {
    float v[16];
#pragma unroll
    for (int k = 0; k < 4; ++k) { v[2 * k] = bf2f(xr[0][k]); v[2 * k + 1] = bfhi(xr[0][k]); v[8 + 2 * k] = bf2f(xr[1][k]); v[8 + 2 * k + 1] = bfhi(xr[1][k]); }
    float s = 0.f;
#pragma unroll
    for (int i = 0; i < 16; ++i) s += v[i];
    s = x32_sum(x16_sum(row_sum(s)));
    const float mean = s * (1.f / 1024.f);
    float q = 0.f;
#pragma unroll
    for (int i = 0; i < 16; ++i) { const float d = v[i] - mean; q += d * d; }
    q = x32_sum(x16_sum(row_sum(q)));
    const float var = rbf(q * (1.f / 1024.f));
    const float mb = rbf(mean);
    const float den = rbf(sqrtf(rbf(var + 1e-5f)));
    const float rden = 1.f / den;
    unsigned* xd = (unsigned*)(smem + L_XD) + w * 1024 + lane * 16;
#pragma unroll
    for (int k = 0; k < 4; ++k) {
      v4u o;
#pragma unroll
      for (int i = 0; i < 4; ++i) { const float t = rbf(v[4 * k + i] - mb) * rden; o[i] = cvtpk(t, t); }
      *(v4u*)(xd + 4 * k) = o;
    }
  }
  if (tid == 0) *(unsigned*)(smem + L_MISC) = 0u;
  WG_BARRIER();
  STAMP(1);
  const unsigned tag = __builtin_amdgcn_readfirstlane(ep) + 1u;
  gemv64(tq, (const unsigned*)(smem + L_XD) + 256 * ns + 32 * w, mk, (float*)(smem + L_RQ) + w * 128, lane);
  WG_BARRIER();
  if (w == 0 && lane < 32) {
    const float* red = (const float*)(smem + L_RQ) + (lane >> 4) * 64 + 4 * (lane & 15);
    f32x4 s = {0.f, 0.f, 0.f, 0.f};
#pragma unroll
    for (int ww = 0; ww < 8; ++ww) s += *(const f32x4*)(red + ww * 128);
    STSC(gran4(s, tag), rs_ws, WS_QP + (unsigned)((ns * 2 + (lane >> 4)) * 1024 + ks * 16 + (lane & 15)) * 16u);
  }
  SB();
#pragma unroll
  for (int i = 0; i < 4; ++i) a[i] = ITEM(i);
  SB();
  STAMP(2);
  gemv64(t1, (const unsigned*)(smem + L_XD) + 256 * ns + 32 * w, mk, (float*)(smem + L_RH) + w * 128, lane);
  WG_BARRIER();
  if (w == 1 && lane < 32 && has_ffn) {
    const float* red = (const float*)(smem + L_RH) + (lane >> 4) * 64 + 4 * (lane & 15);
    f32x4 s = {0.f, 0.f, 0.f, 0.f};
#pragma unroll
    for (int ww = 0; ww < 8; ++ww) s += *(const f32x4*)(red + ww * 128);
    STSC(gran4(s, tag), rs_ws, WS_HP + (unsigned)((ns * 2 + (lane >> 4)) * 896 + ks * 16 + (lane & 15)) * 16u);
  }
  SB();
#pragma unroll
  for (int i = 4; i < EARLY_N; ++i) a[i] = ITEM(i);
  SB();
  STAMP(3);
  {
    const unsigned qo = WS_QP + (unsigned)(((tid & 1) * 4 + bb) * 1024 + kvh * 256 + (tid >> 1)) * 16u;
    v4u gq[2];
    WAIT_TICK(tstart, QG_T);
    gq[0] = LDSC(rs_ws, qo); gq[1] = LDSC(rs_ws, qo + 2 * 1024 * 16);
    SB();
#pragma unroll
    for (int i = EARLY_N; i < EARLY_N + 8; ++i) a[i] = ITEM(i);
    SB();
    bool ok = g4_ok(gq[0], tag) & g4_ok(gq[1], tag);
    STAMP(6);
    if (!__all(ok)) {
      unsigned spins = 0;
      do {
        __builtin_amdgcn_s_sleep(2); MEMFENCE();
        if (!ok) { gq[0] = LDSC(rs_ws, qo); gq[1] = LDSC(rs_ws, qo + 2 * 1024 * 16); }
        VM0();
        ok = g4_ok(gq[0], tag) & g4_ok(gq[1], tag);
      } while (!__all(ok) && ++spins < SPIN_CAP);
    }
    f32x4 s = g4_val(gq[0]) + g4_val(gq[1]);
#pragma unroll
    for (int k = 0; k < 4; ++k) s[k] += dpp<0xB1>(s[k]);
    ((unsigned*)(smem + L_QS))[tid] = (tid & 1) ? cvtpk(s[2], s[3]) : cvtpk(s[0], s[1]);
  }
  WG_BARRIER();
  STAMP(4);
#pragma unroll
  for (int i = EARLY_N + 8; i < W; ++i) a[i] = ITEM(i);
  SB();
  const int headn = 4 * (n16 & 1) + (n16 >> 2), bpp = (n16 >> 1) & 1;
  v4u qb[2];
#pragma unroll
  for (int t = 0; t < 2; ++t) qb[t] = *(const v4u*)(smem + L_QS + headn * 256 + (4 * g + 2 * t + bpp) * 16);
  const unsigned ho = WS_HP + (unsigned)(((lane & 1) * 4 + (lane >> 5)) * 896 + ks * 16 + ((lane >> 1) & 15)) * 16u;
  v4u gh[2];
  {
    const bool hact = (w == 2 && has_ffn);
    gh[0] = LDSC(rs_ws, hact ? ho : OOB); gh[1] = LDSC(rs_ws, hact ? ho + 2 * 896 * 16 : OOB);
  }
  SB();
  float S[NKV];
#pragma unroll
  for (int i = 0; i < NKV; ++i) {
    const v4u cur = a[i % W];
    if (i + RD >= W) { PACE(i + RD); a[(i + RD) % W] = ITEM(i + RD); }
    const f32x4 z = {0.f, 0.f, 0.f, 0.f};
    const f32x4 d0 = mfma(cur, qb[0], z), d1 = mfma(cur, qb[1], z);
    const float t1_ = d0[0] + d1[2], t2_ = d0[1] + d1[3];
    const float x_ = bpp ? t2_ : t1_;
    S[i] = x_ + dpp<0x4E>(x_);
    SB();
  }
  STAMP(5);
  float mr;
  {
    float mx = S[0];
#pragma unroll
    for (int i = 1; i < NKV; ++i) mx = fmaxf(mx, S[i]);
    mx = x32_max(x16_max(mx));
    mr = rbf(rbf(mx) * SCALE);
  }
  v4u tw[8];
  const __amdgpu_buffer_rsrc_t rs_w2 = mkrs(w2, has_ffn ? 3584u * 2048u : 0u);
  const __amdgpu_buffer_rsrc_t rs_wo = mkrs(wo, 4096u * 2048u);
  const unsigned wvo = (unsigned)(64 * ks + 8 * w + g) * 2048u + ns * 512 + n16 * 16;
#define WLD(k) ((k) < 4 ? LDNT(rs_w2, wvo, (((k) >> 1) & 1) * 4 * 2048 + ((k) & 1) * 256) : LDNT(rs_wo, wvo, (((k) >> 1) & 1) * 4 * 2048 + ((k) & 1) * 256))
  float lsum = 0.f;
  const float mc = mr * LOG2E;
  unsigned P[NKV];
#pragma unroll
  for (int i = 0; i < NKV; ++i) {
    if ((i & 3) == 0 && (i >> 2) < TOPUP) { PACE(NKV + RD + (i >> 2)); a[(NKV + RD + (i >> 2)) % W] = ITEM(NKV + RD + (i >> 2)); SB(); }
    const float sr = rbf(rbf(S[i]) * SCALE);
    const float p = __builtin_amdgcn_exp2f(sr * LOG2E - mc);
    lsum += p;
    P[i] = cvtpk(p, p);
  }
  lsum = x32_sum(x16_sum(lsum));
  if (g == 0 && bpp == 0) { ((float*)(smem + L_MX))[headn * 8 + w] = mr; ((float*)(smem + L_LS))[headn * 8 + w] = lsum; }
  asm volatile("s_waitcnt lgkmcnt(0)" ::: "memory");
  if (lane == 0) __hip_atomic_fetch_add((LAS unsigned*)(smem + L_MISC), 1u, __ATOMIC_RELAXED, __HIP_MEMORY_SCOPE_WORKGROUP);
  SB();
  f32x4 acc[4];
#pragma unroll
  for (int t = 0; t < 4; ++t) acc[t] = f32x4{0.f, 0.f, 0.f, 0.f};
  v4u gml[4];
  const unsigned mlo = WS_ML + (unsigned)((pair * 32 + (lane & 31)) * 8 + (lane >> 5)) * 16u;
#define VSTEPS(lo, hi) _Pragma("unroll") for (int i = NKV + (lo); i < NKV + (hi); ++i) { \
    const v4u cur = a[i % W]; \
    if (i + RD < 2 * NKV && i + RD >= NKV + RD + TOPUP) { PACE(i + RD); a[(i + RD) % W] = ITEM(i + RD); } \
    if (i >= 2 * NKV - W_IN_LOOP) tw[i - (2 * NKV - W_IN_LOOP)] = WLD(i - (2 * NKV - W_IN_LOOP)); \
    const unsigned pp = P[i - NKV]; \
    const unsigned p0 = dppu<0x00>(pp), p1 = dppu<0x55>(pp); \
    acc[0] = mfma16(p0 & mk2[0], p0 & mk2[1], cur.x, cur.y, acc[0]); \
    acc[1] = mfma16(p0 & mk2[0], p0 & mk2[1], cur.z, cur.w, acc[1]); \
    acc[2] = mfma16(p1 & mk2[0], p1 & mk2[1], cur.x, cur.y, acc[2]); \
    acc[3] = mfma16(p1 & mk2[0], p1 & mk2[1], cur.z, cur.w, acc[3]); \
    SB(); \
  }
  static_assert(ML_PUB_STEP <= H_USE_STEP && H_USE_STEP <= ML_GET_STEP && ML_GET_STEP <= ML_USE_STEP && ML_USE_STEP <= 32, "order of the side jobs");
  VSTEPS(0, ML_PUB_STEP)
  WG_BARRIER();
  if (w == 7 && g == 0 && bpp == 0) {
    const f32x4 m0 = *(const f32x4*)(smem + L_MX + headn * 32), m1 = *(const f32x4*)(smem + L_MX + headn * 32 + 16);
    const f32x4 l0 = *(const f32x4*)(smem + L_LS + headn * 32), l1 = *(const f32x4*)(smem + L_LS + headn * 32 + 16);
    const float M = fmaxf(fmaxf(fmaxf(m0[0], m0[1]), fmaxf(m0[2], m0[3])), fmaxf(fmaxf(m1[0], m1[1]), fmaxf(m1[2], m1[3])));
    float L = 0.f;
#pragma unroll
    for (int k = 0; k < 4; ++k) L += __builtin_amdgcn_exp2f((m0[k] - M) * LOG2E) * l0[k] + __builtin_amdgcn_exp2f((m1[k] - M) * LOG2E) * l1[k];
    STSC(gran2(M, L, tag), rs_ws, WS_ML + (unsigned)((pair * 32 + sp) * 8 + headn) * 16u);
  }
  SB();
  VSTEPS(ML_PUB_STEP, H_USE_STEP)
  if (w == 2) {
    bool ok = !has_ffn | (g4_ok(gh[0], tag) & g4_ok(gh[1], tag));
    if (!__all(ok)) {
      unsigned spins = 0;
      do {
        __builtin_amdgcn_s_sleep(2); MEMFENCE();
        gh[0] = LDSC(rs_ws, ho); gh[1] = LDSC(rs_ws, ho + 2 * 896 * 16);
        VM0();
        ok = !has_ffn | (g4_ok(gh[0], tag) & g4_ok(gh[1], tag));
      } while (!__all(ok) && ++spins < SPIN_CAP);
    }
    f32x4 s = g4_val(gh[0]) + g4_val(gh[1]);
#pragma unroll
    for (int k = 0; k < 4; ++k) s[k] += dpp<0xB1>(s[k]);
    const float h0 = rbf((lane & 1) ? s[2] : s[0]), h1 = rbf((lane & 1) ? s[3] : s[1]);
    const float s0 = h0 / (1.f + __expf(-h0)), s1 = h1 / (1.f + __expf(-h1));
    v2u o; o.x = cvtpk(s0, s0); o.y = cvtpk(s1, s1);
    if (!has_ffn) { o.x = 0u; o.y = 0u; }
    *(v2u*)(smem + L_HD + ((lane >> 5) * 64 + 2 * (lane & 31)) * 4) = o;
  }
  SB();
  VSTEPS(H_USE_STEP, ML_GET_STEP)
#pragma unroll
  for (int k = 0; k < 4; ++k) gml[k] = LDSC(rs_ws, w == 5 ? mlo + k * 32 : OOB);
  SB();
  VSTEPS(ML_GET_STEP, ML_USE_STEP)
  if (w == 5) {
    bool ok = tags_ok(gml[0], tag) & tags_ok(gml[1], tag) & tags_ok(gml[2], tag) & tags_ok(gml[3], tag);
    if (!__all(ok)) {
      unsigned spins = 0;
      do {
        __builtin_amdgcn_s_sleep(2); MEMFENCE();
#pragma unroll
        for (int k = 0; k < 4; ++k) gml[k] = LDSC(rs_ws, mlo + k * 32);
        VM0();
        ok = tags_ok(gml[0], tag) & tags_ok(gml[1], tag) & tags_ok(gml[2], tag) & tags_ok(gml[3], tag);
      } while (!__all(ok) && ++spins < SPIN_CAP);
    }
#pragma unroll
    for (int k = 0; k < 4; ++k) {
      const float m = __uint_as_float(gml[k].x), l = __uint_as_float(gml[k].z);
      const float M = x16_max(row_max(m));
      const float L = x16_sum(row_sum(__builtin_amdgcn_exp2f((m - M) * LOG2E) * l));
      const int e = (2 * k + (lane >> 5)) * 8 + (lane & 7);
      if ((lane & 31) < 8) ((float*)(smem + L_FS))[e] = __builtin_amdgcn_exp2f((((const float*)(smem + L_MX))[e] - M) * LOG2E) / L;
    }
  }
  SB();
  VSTEPS(ML_USE_STEP, NKV)
  STAMP(7);
#pragma unroll
  for (int t = 0; t < 4; ++t) *(f32x4*)(smem + L_MRG + ((w * 4 + t) * 64 + lane) * 16) = acc[t];
  WG_BARRIER();
  STAMP(8);
  if (tid < 256) {
    const int hl = tid >> 5, dq = tid & 31;
    const unsigned char* src = smem + L_MRG + ((2 * (hl >> 2) + (dq & 1)) * 64 + (hl & 3) * 16 + (dq >> 1)) * 16;
    const f32x4 c0 = *(const f32x4*)(smem + L_FS + hl * 32), c1 = *(const f32x4*)(smem + L_FS + hl * 32 + 16);
    f32x4 s = {0.f, 0.f, 0.f, 0.f};
#pragma unroll
    for (int ww = 0; ww < 8; ++ww) s += (ww < 4 ? c0[ww & 3] : c1[ww & 3]) * *(const f32x4*)(src + ww * 4096);
    STSC(gran4(s, tag), rs_ws, WS_OP + (unsigned)j * OP_STR + (unsigned)tid * 16u);
  }
  SB();
  STAMP(9);
  const unsigned tpub = (unsigned)__builtin_amdgcn_s_memrealtime();
#pragma unroll
  for (int k = W_IN_LOOP; k < 8; ++k) tw[k] = WLD(k);
  SB();
  STAMP(10);
  const unsigned opo = WS_OP + (unsigned)(((w >> 2) * 128 + (ks >> 4) * 32 + 8 * (w & 3) + g) * OP_STR) + (unsigned)(((ks >> 1) & 7) * 512 + (ks & 1) * 256 + n16 * 16);
  v4u go[2];
  WAIT_TICK(tpub, OG_DELAY);
  STAMP(12);
  go[0] = LDSC(rs_ws, opo); go[1] = LDSC(rs_ws, opo + 4 * OP_STR);
  SB();
  f32x4 facc[2];
  {
    const unsigned* hd = (const unsigned*)(smem + L_HD) + ((lane >> 3) & 1) * 64 + 8 * w + g;
    const unsigned h0 = hd[0], h1 = hd[4];
    const v4u A0 = {h0 & mk[0], h0 & mk[1], h0 & mk[2], h0 & mk[3]}, A1 = {h1 & mk[0], h1 & mk[1], h1 & mk[2], h1 & mk[3]};
    facc[0] = mfma(A1, tw[2], mfma(A0, tw[0], f32x4{0.f, 0.f, 0.f, 0.f}));
    facc[1] = mfma(A1, tw[3], mfma(A0, tw[1], f32x4{0.f, 0.f, 0.f, 0.f}));
  }
  asm volatile("" :: "v"(facc[0]), "v"(facc[1]));
  SB();
  STAMP(11);
  {
    POLL2(go, opo, 4 * OP_STR);
    STAMP(13);
    f32x4 o = g4_val(go[0]) + g4_val(go[1]);
#pragma unroll
    for (int k = 0; k < 4; ++k) o[k] = x32_sum(x16_sum(o[k]));
    if (lane < 16) *(f32x4*)(smem + L_RQ + w * 256 + lane * 16) = o;
    WG_BARRIER();
    const float* os = (const float*)(smem + L_RQ) + ((lane >> 3) & 1) * 256 + 8 * w + g;
    const float v0 = (os[0] + os[64]) + (os[128] + os[192]), v1 = (os[4] + os[68]) + (os[132] + os[196]);
    const unsigned od0 = cvtpk(v0, v0), od1 = cvtpk(v1, v1);
    const v4u A0 = {od0 & mk[0], od0 & mk[1], od0 & mk[2], od0 & mk[3]}, A1 = {od1 & mk[0], od1 & mk[1], od1 & mk[2], od1 & mk[3]};
    const f32x4 r0 = mfma(A1, tw[6], mfma(A0, tw[4], facc[0]));
    const f32x4 r1 = mfma(A1, tw[7], mfma(A0, tw[5], facc[1]));
    *(f32x4*)(smem + L_FIN + ((w * 2 + 0) * 64 + lane) * 16) = r0;
    *(f32x4*)(smem + L_FIN + ((w * 2 + 1) * 64 + lane) * 16) = r1;
  }
  WG_BARRIER();
  if (tid < 128) {
    const int b = tid >> 6, cq = tid & 63;
    const unsigned char* src = smem + L_FIN + ((cq >> 5) * 64 + (2 * b + (cq & 1)) * 16 + ((cq & 31) >> 1)) * 16;
    f32x4 s = {0.f, 0.f, 0.f, 0.f};
#pragma unroll
    for (int ww = 0; ww < 8; ++ww) s += *(const f32x4*)(src + ww * 2048);
    STSC(gran4(s, tag), rs_ws, WS_FP + (unsigned)j * FP_STR + (unsigned)tid * 16u);
  }
  STAMP(14);
  if (w == 7) {
    const unsigned fo = WS_FP + (unsigned)((((j >> 5) & 3) * 64 + (lane >> 1)) * FP_STR) + (unsigned)((j >> 7) * 1024 + (j & 31) * 32 + (lane & 1) * 16);
    { const unsigned tf = (unsigned)__builtin_amdgcn_s_memrealtime(); WAIT_TICK(tf, FG_DELAY); }
    v4u gf[2];
    gf[0] = LDSC(rs_ws, fo); gf[1] = LDSC(rs_ws, fo + 32 * FP_STR);
    POLL2(gf, fo, 32 * FP_STR);
    f32x4 s = g4_val(gf[0]) + g4_val(gf[1]);
#pragma unroll
    for (int k = 0; k < 4; ++k) s[k] = par_sum(s[k]);
    if (lane < 2) {
      v2u o;
      o.x = cvtpk(bf2f(xres.x) + s[0], bfhi(xres.x) + s[1]);
      o.y = cvtpk(bf2f(xres.y) + s[2], bfhi(xres.y) + s[3]);
      ((v2u*)out)[(j >> 7) * 256 + (j & 127) * 2 + lane] = o;
    }
    if (j == 0 && lane == 0) __builtin_amdgcn_raw_buffer_store_b32(tag, rs_ws, WS_E, 0, AUX_SC1);
    STAMP(15);
  }
  STAMP_FLUSH();
}

extern "C" void kernel_launch(void* const* d_in, const int* in_sizes, int n_in, void* d_out, int out_size, void* d_ws, size_t ws_size,
                              hipStream_t stream) {
  fused_decode<<<NWG, NTHR, 0, stream>>>((const u16*)d_in[0], (const u16*)d_in[1], (const u16*)d_in[2], (const u16*)d_in[3],
                                         (const u16*)d_in[4], (const u16*)d_in[6], (const u16*)d_in[7], (u16*)d_out,
                                         (unsigned char*)d_ws);
}
```

```cpp
#include <hip/hip_runtime.h>
#include <stdint.h>

typedef unsigned v4u __attribute__((ext_vector_type(4)));
typedef unsigned v2u __attribute__((ext_vector_type(2)));
typedef float f32x4 __attribute__((ext_vector_type(4)));
typedef float f32x2 __attribute__((ext_vector_type(2)));
typedef short bf16x8 __attribute__((ext_vector_type(8)));
typedef short s16x4 __attribute__((ext_vector_type(4)));
typedef __bf16 bf16x2 __attribute__((ext_vector_type(2)));
typedef unsigned short u16;

#define PAT_C 4
#define PAT_STG 4
#define PAT_ROT 1
#define RING_W 20
#define RING_D 8
#define EARLY_N 8
#define QG_T 460
#define W_IN_LOOP 3
#define OG_DELAY 120
#define FG_DELAY 70
#define POLL_HALF 10
#define POLL_ROUNDS 4
#define ML_PUB_STEP 4
#define H_USE_STEP 8
#define ML_GET_STEP 14
#define ML_USE_STEP 26
#define EARLY_ARGS 1
#define TAU16 0
#define PACE_P0 840
#define QG_MID 4
#define PAD_B 256

#define BIAS 0
constexpr int NWG = 256, NTHR = 512, NKV = 32 + BIAS;
constexpr int W = RING_W, RD = RING_D;
constexpr int TOPUP = (W - RD) < 8 ? (W - RD) : 8;
constexpr unsigned OP_STR = 4096 + PAD_B, FP_STR = 2048 + PAD_B;
constexpr unsigned WS_E = 0;
constexpr unsigned WS_QP = 4096;
constexpr unsigned WS_HP = WS_QP + 4 * 2 * 1024 * 16;
constexpr unsigned WS_ML = WS_HP + 4 * 2 * 896 * 16;
constexpr unsigned WS_OP = WS_ML + 8 * 32 * 8 * 16;
constexpr unsigned WS_FP = WS_OP + 256 * OP_STR;
constexpr unsigned WS_END = WS_FP + 256 * FP_STR;
constexpr unsigned WS_STAMP = WS_END;
constexpr int L_XD = 0, L_RQ = 8192, L_RH = 12288, L_QS = 16384, L_HD = 18432, L_MX = 18944, L_LS = 19200, L_FS = 19456, L_MISC = 19712,
              L_MRG = 19776, L_FIN = L_MRG + 32768, L_END = L_FIN + 32768;
constexpr unsigned SPIN_CAP = 1u << 16;
constexpr int AUX_NT = 2, AUX_SC1 = 16;
constexpr unsigned OOB = 0xFFFFFF00u;
constexpr float SCALE = 0.08838834764831845f, LOG2E = 1.4426950408889634f;

__device__ __forceinline__ float bf2f(unsigned b) { return __builtin_bit_cast(float, b << 16); }
__device__ __forceinline__ float bfhi(unsigned b) { return __builtin_bit_cast(float, b & 0xffff0000u); }
__device__ __forceinline__ unsigned cvtpk(float lo, float hi) {
  return __builtin_bit_cast(unsigned, __builtin_convertvector(f32x2{lo, hi}, bf16x2));
}
__device__ __forceinline__ float rbf(float x) { return bfhi(cvtpk(x, x)); }
__device__ __forceinline__ f32x4 mfma(v4u a, v4u b, f32x4 c) {
  return __builtin_amdgcn_mfma_f32_16x16x32_bf16(__builtin_bit_cast(bf16x8, a), __builtin_bit_cast(bf16x8, b), c, 0, 0, 0);
}
__device__ __forceinline__ f32x4 mfma16(unsigned a0, unsigned a1, unsigned b0, unsigned b1, f32x4 c) {
  return __builtin_amdgcn_mfma_f32_16x16x16bf16_1k(__builtin_bit_cast(s16x4, v2u{a0, a1}), __builtin_bit_cast(s16x4, v2u{b0, b1}), c, 0, 0, 0);
}
template <int CTRL> __device__ __forceinline__ float dpp(float x) {
  return __builtin_bit_cast(float, __builtin_amdgcn_mov_dpp(__builtin_bit_cast(int, x), CTRL, 0xf, 0xf, true));
}
template <int CTRL> __device__ __forceinline__ unsigned dppu(unsigned x) { return (unsigned)__builtin_amdgcn_mov_dpp((int)x, CTRL, 0xf, 0xf, true); }
__device__ __forceinline__ float x16_max(float x) {
  auto s = __builtin_amdgcn_permlane16_swap(__float_as_uint(x), __float_as_uint(x), false, false);
  return fmaxf(__uint_as_float(s[0]), __uint_as_float(s[1]));
}
__device__ __forceinline__ float x16_sum(float x) {
  auto s = __builtin_amdgcn_permlane16_swap(__float_as_uint(x), __float_as_uint(x), false, false);
  return __uint_as_float(s[0]) + __uint_as_float(s[1]);
}
__device__ __forceinline__ float x32_max(float x) {
  auto s = __builtin_amdgcn_permlane32_swap(__float_as_uint(x), __float_as_uint(x), false, false);
  return fmaxf(__uint_as_float(s[0]), __uint_as_float(s[1]));
}
__device__ __forceinline__ float x32_sum(float x) {
  auto s = __builtin_amdgcn_permlane32_swap(__float_as_uint(x), __float_as_uint(x), false, false);
  return __uint_as_float(s[0]) + __uint_as_float(s[1]);
}
__device__ __forceinline__ float row_sum(float x) {
  x += dpp<0xB1>(x); x += dpp<0x4E>(x); x += dpp<0x141>(x); x += dpp<0x140>(x); return x;
}
__device__ __forceinline__ float row_max(float x) {
  x = fmaxf(x, dpp<0xB1>(x)); x = fmaxf(x, dpp<0x4E>(x)); x = fmaxf(x, dpp<0x141>(x)); x = fmaxf(x, dpp<0x140>(x)); return x;
}
__device__ __forceinline__ float par_sum(float x) {
  x += dpp<0x4E>(x); x += dpp<0x124>(x); x += dpp<0x128>(x); return x32_sum(x16_sum(x));
}
#define LAS __attribute__((address_space(3)))
#define WG_BARRIER() do { asm volatile("s_waitcnt lgkmcnt(0)" ::: "memory"); __builtin_amdgcn_s_barrier(); asm volatile("" ::: "memory"); } while (0)
#define SB() __builtin_amdgcn_sched_barrier(0)
#define VM0() __builtin_amdgcn_s_waitcnt(0x0F70)
#define MEMFENCE() asm volatile("" :: "s"(ws) : "memory")
#define LDNT(rs, vo, so) __builtin_amdgcn_raw_buffer_load_b128(rs, vo, so, AUX_NT)
#define LDSC(rs, vo) __builtin_amdgcn_raw_buffer_load_b128(rs, vo, 0, AUX_SC1)
#define STSC(v, rs, vo) __builtin_amdgcn_raw_buffer_store_b128(v, rs, vo, 0, AUX_SC1)
#define WAIT_TICK(base, dt) do { unsigned d_; \
    asm volatile("s_branch 2f\n1: s_sleep 1\n2: s_memrealtime vcc\n\ts_waitcnt lgkmcnt(0)\n\ts_sub_u32 %0, vcc_lo, %1\n\ts_cmp_lt_u32 %0, %2\n\ts_cbranch_scc1 1b" \
                 : "=&s"(d_) : "s"(base), "n"(dt) : "vcc", "scc"); } while (0)
#define WAIT_WAVES() do { unsigned n_ = 0; while (__hip_atomic_load((LAS unsigned*)(smem + L_MISC), __ATOMIC_RELAXED, __HIP_MEMORY_SCOPE_WORKGROUP) < 8u && ++n_ < SPIN_CAP) __builtin_amdgcn_s_sleep(1); asm volatile("" ::: "memory"); } while (0)
__device__ __forceinline__ __amdgpu_buffer_rsrc_t mkrs(const void* p, unsigned bytes) {
  return __builtin_amdgcn_make_buffer_rsrc((void*)p, 0, (int)bytes, 0x00020000);
}
__device__ __forceinline__ bool tags_ok(v4u v, unsigned tag) { return (v.y == tag) & (v.w == tag); }
__device__ __forceinline__ v4u gran2(float a, float b, unsigned tag) {
  return v4u{__builtin_bit_cast(unsigned, a), tag, __builtin_bit_cast(unsigned, b), tag};
}
__device__ __forceinline__ v4u gran4(f32x4 v, unsigned tag) {
  v4u r;
#pragma unroll
  for (int k = 0; k < 4; ++k) r[k] = ((__float_as_uint(v[k]) + 0x80u) & 0xFFFFFF00u) | ((tag >> (8 * k)) & 0xFFu);
  return r;
}
__device__ __forceinline__ bool g4_ok(v4u v, unsigned tag) {
  const unsigned lo = __builtin_amdgcn_perm(v.y, v.x, 0x0c0c0400u), hi = __builtin_amdgcn_perm(v.w, v.z, 0x04000c0cu);
  return (lo | hi) == tag;
}
__device__ __forceinline__ f32x4 g4_val(v4u v) {
  return f32x4{__uint_as_float(v.x & 0xFFFFFF00u), __uint_as_float(v.y & 0xFFFFFF00u), __uint_as_float(v.z & 0xFFFFFF00u), __uint_as_float(v.w & 0xFFFFFF00u)};
}
#define PQ_ISSUE(X, BASE, STR) do { X[0] = LDSC(rs_ws, done_ ? OOB : (BASE)); X[1] = LDSC(rs_ws, done_ ? OOB : (BASE) + (STR)); } while (0)
#define PQ_TAKE(G, X) do { \
    asm volatile("" : "+v"(X[0]), "+v"(X[1]) :: "memory"); \
    const bool c_ = !done_ & g4_ok(X[0], tag) & g4_ok(X[1], tag); \
    _Pragma("unroll") for (int k_ = 0; k_ < 4; ++k_) { G[0][k_] = c_ ? X[0][k_] : G[0][k_]; G[1][k_] = c_ ? X[1][k_] : G[1][k_]; } \
    done_ |= c_; } while (0)
#define POLL2(G, BASE, STR) do { \
    bool done_ = g4_ok(G[0], tag) & g4_ok(G[1], tag); \
    if (__all(done_)) break; \
    v4u X_[2], Y_[2]; \
    MEMFENCE(); PQ_ISSUE(X_, BASE, STR); \
    __builtin_amdgcn_s_sleep(POLL_HALF); \
    MEMFENCE(); PQ_ISSUE(Y_, BASE, STR); \
    bool fin_ = false; \
    _Pragma("unroll") for (int r_ = 0; r_ < POLL_ROUNDS; ++r_) { \
      PQ_TAKE(G, X_); \
      if (__all(done_)) { fin_ = true; break; } \
      MEMFENCE(); PQ_ISSUE(X_, BASE, STR); \
      PQ_TAKE(G, Y_); \
      if (__all(done_)) { fin_ = true; break; } \
      MEMFENCE(); PQ_ISSUE(Y_, BASE, STR); \
    } \
    if (fin_) break; \
    VM0(); \
    for (unsigned spins_ = 0; spins_ < SPIN_CAP; ++spins_) { \
      PQ_TAKE(G, X_); \
      if (__all(done_)) break; \
      __builtin_amdgcn_s_sleep(2); \
      MEMFENCE(); PQ_ISSUE(X_, BASE, STR); \
      VM0(); \
    } \
  } while (0)
#define STAMP(i) do { } while (0)
#define STAMP_FLUSH() do { } while (0)

__device__ __forceinline__ void gemv64(const v4u (&wv)[4], const unsigned* xd  , const unsigned (&mk)[4],
                                       float* red  , int lane) {
  const int g = lane >> 4, bsel = (lane >> 3) & 1;
  f32x4 a0 = {0.f, 0.f, 0.f, 0.f}, a1 = {0.f, 0.f, 0.f, 0.f};
#pragma unroll
  for (int u = 0; u < 4; ++u) {
    const v2u x2 = *(const v2u*)(xd + bsel * 1024 + 8 * u + 2 * g);
    a0 = mfma(v4u{x2.x & mk[0], x2.x & mk[1], x2.x & mk[2], x2.x & mk[3]}, wv[u], a0);
    a1 = mfma(v4u{x2.y & mk[0], x2.y & mk[1], x2.y & mk[2], x2.y & mk[3]}, wv[u], a1);
  }
  f32x4 r;
#pragma unroll
  for (int i = 0; i < 4; ++i) { const float v = bsel ? a1[i] : a0[i]; r[i] = v + dpp<0x128>(v); }
  if (bsel == 0) *(f32x4*)(red + (g >> 1) * 64 + 8 * (lane & 7) + 4 * (g & 1)) = r;
}

__global__ void __launch_bounds__(NTHR, 2)
fused_decode(const u16* __restrict__ x, const u16* __restrict__ kc, const u16* __restrict__ vc, const u16* __restrict__ wq,
             const u16* __restrict__ wo, const u16* __restrict__ w1, const u16* __restrict__ w2, u16* __restrict__ out,
             unsigned char* ws) {
  __shared__ __attribute__((aligned(16))) unsigned char smem[L_END];
  const int tid = threadIdx.x, lane = tid & 63, w = __builtin_amdgcn_readfirstlane(tid >> 6);
  const int j = blockIdx.x;
  const int g = lane >> 4, n16 = lane & 15;
  if (EARLY_ARGS) asm volatile("" :: "s"(x), "s"(kc), "s"(vc), "s"(wq), "s"(wo), "s"(w1), "s"(w2), "s"(out), "s"(ws));
  STAMP(0);
  const unsigned tstart = (unsigned)__builtin_amdgcn_s_memrealtime();
  const __amdgpu_buffer_rsrc_t rs_ws = mkrs(ws, WS_END);
  const int ks = j & 63, ns = j >> 6;
  const bool has_ffn = ks < 56;
  const unsigned ep = __builtin_amdgcn_raw_buffer_load_b32(rs_ws, WS_E, 0, AUX_SC1);
  const __amdgpu_buffer_rsrc_t rs_x = mkrs(x, 4096);
  v4u xr[2];
  {
    const unsigned xo = (w < 2) ? (unsigned)(w * 2048 + lane * 32) : OOB;
    xr[0] = __builtin_amdgcn_raw_buffer_load_b128(rs_x, xo, 0, 0);
    xr[1] = __builtin_amdgcn_raw_buffer_load_b128(rs_x, xo, 16, 0);
  }
  const v2u xres = __builtin_amdgcn_raw_buffer_load_b64(rs_x, (unsigned)((j >> 7) * 2048 + (j & 127) * 16 + (lane & 1) * 8), 0, 0);
  v4u tq[4], t1[4];
  {
    const __amdgpu_buffer_rsrc_t rs = mkrs(wq, 1024u * 8192u);
    const unsigned vo = (unsigned)(256 * ns + 32 * w + (lane >> 3)) * 8192u + ks * 128 + (lane & 7) * 16;
#pragma unroll
    for (int u = 0; u < 4; ++u) tq[u] = LDNT(rs, vo, u * 8 * 8192);
  }
  {
    const __amdgpu_buffer_rsrc_t rs = mkrs(w1, has_ffn ? 1024u * 7168u : 0u);
    const unsigned vo = (unsigned)(256 * ns + 32 * w + (lane >> 3)) * 7168u + ks * 128 + (lane & 7) * 16;
#pragma unroll
    for (int u = 0; u < 4; ++u) t1[u] = LDNT(rs, vo, u * 8 * 7168);
  }
  const int pair = j >> 5, sp = j & 31, bb = pair >> 2, kvh = pair & 3;
  const bool odd = sp & 1;
  const unsigned char* kbase = (const unsigned char*)kc + (size_t)pair * (8u << 20);
  const unsigned char* vbase = (const unsigned char*)vc + (size_t)pair * (8u << 20);
  const __amdgpu_buffer_rsrc_t rs_k = mkrs(kbase, 8u << 20), rs_v = mkrs(vbase, 8u << 20);
  const __amdgpu_buffer_rsrc_t rs_k2 = mkrs(kbase, odd ? 0u : (8u << 20)), rs_v2 = mkrs(vbase, odd ? 0u : (8u << 20));
  const unsigned wbase = (unsigned)(sp * 8 + w) * PAT_C * 1024u;
  const unsigned kvo = wbase + ((lane >> 2) & 3) * 256 + (4 * g + (lane & 3)) * 16;
  const unsigned vvo = wbase + lane * 16;
  const int st = pair * PAT_STG + (sp * 8 + w) * PAT_ROT;
#define UOFF(n, st_) ((unsigned)((((n) + (st_)) & 31) / PAT_C) * (256u * PAT_C * 1024u) + (unsigned)((((n) + (st_)) & 31) % PAT_C) * 1024u)
#define KVLD(rs, rs2, vo, i) ((i) < 32 - BIAS ? LDNT(rs, vo, UOFF(i, st)) : (i) < 32 ? LDNT(rs2, vo, UOFF(i, st)) : LDNT(rs2, vo, UOFF((i) - BIAS, st + 8 * PAT_ROT) + 8u * PAT_C * 1024u))
#define ITEM(i) ((i) < NKV ? KVLD(rs_k, rs_k2, kvo, i) : KVLD(rs_v, rs_v2, vvo, (i) - NKV))
#define PACE(i) do { if (TAU16 && (((i) - W) & 3) == 0) WAIT_TICK(tstart, PACE_P0 + ((((i) - W) * TAU16) >> 4)); } while (0)
  v4u a[W];
  SB();
  unsigned mk[4], mk2[2];
  {
    const int jp = lane & 7;
#pragma unroll
    for (int k = 0; k < 4; ++k) mk[k] = ((jp >> 1) == k) ? ((jp & 1) ? 0xffff0000u : 0x0000ffffu) : 0u;
#pragma unroll
    for (int k = 0; k < 2; ++k) mk2[k] = (((jp & 3) >> 1) == k) ? ((jp & 1) ? 0xffff0000u : 0x0000ffffu) : 0u;
  }
  if (w < 2) {
    float v[16];
#pragma unroll
    for (int k = 0; k < 4; ++k) { v[2 * k] = bf2f(xr[0][k]); v[2 * k + 1] = bfhi(xr[0][k]); v[8 + 2 * k] = bf2f(xr[1][k]); v[8 + 2 * k + 1] = bfhi(xr[1][k]); }
    float s = 0.f;
#pragma unroll
    for (int i = 0; i < 16; ++i) s += v[i];
    s = x32_sum(x16_sum(row_sum(s)));
    const float mean = s * (1.f / 1024.f);
    float q = 0.f;
#pragma unroll
    for (int i = 0; i < 16; ++i) { const float d = v[i] - mean; q += d * d; }
    q = x32_sum(x16_sum(row_sum(q)));
    const float var = rbf(q * (1.f / 1024.f));
    const float mb = rbf(mean);
    const float den = rbf(sqrtf(rbf(var + 1e-5f)));
    const float rden = 1.f / den;
    unsigned* xd = (unsigned*)(smem + L_XD) + w * 1024 + lane * 16;
#pragma unroll
    for (int k = 0; k < 4; ++k) {
      v4u o;
#pragma unroll
      for (int i = 0; i < 4; ++i) { const float t = rbf(v[4 * k + i] - mb) * rden; o[i] = cvtpk(t, t); }
      *(v4u*)(xd + 4 * k) = o;
    }
  }
  if (tid == 0) *(unsigned*)(smem + L_MISC) = 0u;
  WG_BARRIER();
  STAMP(1);
  const unsigned epoch = __builtin_amdgcn_readfirstlane(ep) + 1u;
  const unsigned tag = (epoch & 0xFFFFu) * 0x10001u;
  gemv64(tq, (const unsigned*)(smem + L_XD) + 256 * ns + 32 * w, mk, (float*)(smem + L_RQ) + w * 128, lane);
  WG_BARRIER();
  if (w == 0 && lane < 32) {
    const float* red = (const float*)(smem + L_RQ) + (lane >> 4) * 64 + 4 * (lane & 15);
    f32x4 s = {0.f, 0.f, 0.f, 0.f};
#pragma unroll
    for (int ww = 0; ww < 8; ++ww) s += *(const f32x4*)(red + ww * 128);
    STSC(gran4(s, tag), rs_ws, WS_QP + (unsigned)((ns * 2 + (lane >> 4)) * 1024 + ks * 16 + (lane & 15)) * 16u);
  }
  SB();
#pragma unroll
  for (int i = 0; i < 4; ++i) a[i] = ITEM(i);
  SB();
  STAMP(2);
  gemv64(t1, (const unsigned*)(smem + L_XD) + 256 * ns + 32 * w, mk, (float*)(smem + L_RH) + w * 128, lane);
  WG_BARRIER();
  if (w == 1 && lane < 32 && has_ffn) {
    const float* red = (const float*)(smem + L_RH) + (lane >> 4) * 64 + 4 * (lane & 15);
    f32x4 s = {0.f, 0.f, 0.f, 0.f};
#pragma unroll
    for (int ww = 0; ww < 8; ++ww) s += *(const f32x4*)(red + ww * 128);
    STSC(gran4(s, tag), rs_ws, WS_HP + (unsigned)((ns * 2 + (lane >> 4)) * 896 + ks * 16 + (lane & 15)) * 16u);
  }
  SB();
#pragma unroll
  for (int i = 4; i < EARLY_N; ++i) a[i] = ITEM(i);
  SB();
  STAMP(3);
  {
    const unsigned qo = WS_QP + (unsigned)(((tid & 1) * 4 + bb) * 1024 + kvh * 256 + (tid >> 1)) * 16u;
    v4u gq[2];
    WAIT_TICK(tstart, QG_T);
    gq[0] = LDSC(rs_ws, qo); gq[1] = LDSC(rs_ws, qo + 2 * 1024 * 16);
    SB();
#pragma unroll
    for (int i = EARLY_N; i < EARLY_N + QG_MID; ++i) a[i] = ITEM(i);
    SB();
    bool ok = g4_ok(gq[0], tag) & g4_ok(gq[1], tag);
    v4u gr[2];
    gr[0] = LDSC(rs_ws, ok ? OOB : qo); gr[1] = LDSC(rs_ws, ok ? OOB : qo + 2 * 1024 * 16);
    SB();
#pragma unroll
    for (int i = EARLY_N + QG_MID; i < EARLY_N + 8; ++i) a[i] = ITEM(i);
    SB();
    STAMP(6);
    if (!__all(ok)) {
      if (!ok) { gq[0] = gr[0]; gq[1] = gr[1]; }
      ok = g4_ok(gq[0], tag) & g4_ok(gq[1], tag);
      unsigned spins = 0;
      while (!__all(ok) && ++spins < SPIN_CAP) {
        __builtin_amdgcn_s_sleep(2); MEMFENCE();
        if (!ok) { gq[0] = LDSC(rs_ws, qo); gq[1] = LDSC(rs_ws, qo + 2 * 1024 * 16); }
        VM0();
        ok = g4_ok(gq[0], tag) & g4_ok(gq[1], tag);
      }
    }
    f32x4 s = g4_val(gq[0]) + g4_val(gq[1]);
#pragma unroll
    for (int k = 0; k < 4; ++k) s[k] += dpp<0xB1>(s[k]);
    ((unsigned*)(smem + L_QS))[tid] = (tid & 1) ? cvtpk(s[2], s[3]) : cvtpk(s[0], s[1]);
  }
  WG_BARRIER();
  STAMP(4);
#pragma unroll
  for (int i = EARLY_N + 8; i < W; ++i) a[i] = ITEM(i);
  SB();
  const int headn = 4 * (n16 & 1) + (n16 >> 2), bpp = (n16 >> 1) & 1;
  v4u qb[2];
#pragma unroll
  for (int t = 0; t < 2; ++t) qb[t] = *(const v4u*)(smem + L_QS + headn * 256 + (4 * g + 2 * t + bpp) * 16);
  const unsigned ho = WS_HP + (unsigned)(((lane & 1) * 4 + (lane >> 5)) * 896 + ks * 16 + ((lane >> 1) & 15)) * 16u;
  v4u gh[2];
  {
    const bool hact = (w == 2 && has_ffn);
    gh[0] = LDSC(rs_ws, hact ? ho : OOB); gh[1] = LDSC(rs_ws, hact ? ho + 2 * 896 * 16 : OOB);
  }
  SB();
  float S[NKV];
#pragma unroll
  for (int i = 0; i < NKV; ++i) {
    const v4u cur = a[i % W];
    if (i + RD >= W) { PACE(i + RD); a[(i + RD) % W] = ITEM(i + RD); }
    const f32x4 z = {0.f, 0.f, 0.f, 0.f};
    const f32x4 d0 = mfma(cur, qb[0], z), d1 = mfma(cur, qb[1], z);
    const float t1_ = d0[0] + d1[2], t2_ = d0[1] + d1[3];
    const float x_ = bpp ? t2_ : t1_;
    S[i] = x_ + dpp<0x4E>(x_);
    if (i >= 32 - BIAS && odd) S[i] = -INFINITY;
    SB();
  }
  STAMP(5);
  float mr;
  {
    float mx = S[0];
#pragma unroll
    for (int i = 1; i < NKV; ++i) mx = fmaxf(mx, S[i]);
    mx = x32_max(x16_max(mx));
    mr = rbf(rbf(mx) * SCALE);
  }
  v4u tw[8];
  const __amdgpu_buffer_rsrc_t rs_w2 = mkrs(w2, has_ffn ? 3584u * 2048u : 0u);
  const __amdgpu_buffer_rsrc_t rs_wo = mkrs(wo, 4096u * 2048u);
  const unsigned wvo = (unsigned)(64 * ks + 8 * w + g) * 2048u + ns * 512 + n16 * 16;
#define WLD(k) ((k) < 4 ? LDNT(rs_w2, wvo, (((k) >> 1) & 1) * 4 * 2048 + ((k) & 1) * 256) : LDNT(rs_wo, wvo, (((k) >> 1) & 1) * 4 * 2048 + ((k) & 1) * 256))
  float lsum = 0.f;
  const float mc = mr * LOG2E;
  unsigned P[NKV];
#pragma unroll
  for (int i = 0; i < NKV; ++i) {
    if ((i & 3) == 0 && (i >> 2) < TOPUP) { PACE(NKV + RD + (i >> 2)); a[(NKV + RD + (i >> 2)) % W] = ITEM(NKV + RD + (i >> 2)); SB(); }
    const float sr = rbf(rbf(S[i]) * SCALE);
    const float p = __builtin_amdgcn_exp2f(sr * LOG2E - mc);
    lsum += p;
    P[i] = cvtpk(p, p);
  }
  lsum = x32_sum(x16_sum(lsum));
  if (g == 0 && bpp == 0) { ((float*)(smem + L_MX))[headn * 8 + w] = mr; ((float*)(smem + L_LS))[headn * 8 + w] = lsum; }
  asm volatile("s_waitcnt lgkmcnt(0)" ::: "memory");
  if (lane == 0) __hip_atomic_fetch_add((LAS unsigned*)(smem + L_MISC), 1u, __ATOMIC_RELAXED, __HIP_MEMORY_SCOPE_WORKGROUP);
  SB();
  f32x4 acc[4];
#pragma unroll
  for (int t = 0; t < 4; ++t) acc[t] = f32x4{0.f, 0.f, 0.f, 0.f};
  v4u gml[4];
  const unsigned mlo = WS_ML + (unsigned)((pair * 32 + (lane & 31)) * 8 + (lane >> 5)) * 16u;
#define VSTEPS(lo, hi) _Pragma("unroll") for (int i = NKV + (lo); i < NKV + (hi); ++i) { \
    const v4u cur = a[i % W]; \
    if (i + RD < 2 * NKV && i + RD >= NKV + RD + TOPUP) { PACE(i + RD); a[(i + RD) % W] = ITEM(i + RD); } \
    if (i >= 2 * NKV - W_IN_LOOP) tw[i - (2 * NKV - W_IN_LOOP)] = WLD(i - (2 * NKV - W_IN_LOOP)); \
    const unsigned pp = P[i - NKV]; \
    const unsigned p0 = dppu<0x00>(pp), p1 = dppu<0x55>(pp); \
    acc[0] = mfma16(p0 & mk2[0], p0 & mk2[1], cur.x, cur.y, acc[0]); \
    acc[1] = mfma16(p0 & mk2[0], p0 & mk2[1], cur.z, cur.w, acc[1]); \
    acc[2] = mfma16(p1 & mk2[0], p1 & mk2[1], cur.x, cur.y, acc[2]); \
    acc[3] = mfma16(p1 & mk2[0], p1 & mk2[1], cur.z, cur.w, acc[3]); \
    SB(); \
  }
  static_assert(ML_PUB_STEP <= H_USE_STEP && H_USE_STEP <= ML_GET_STEP && ML_GET_STEP <= ML_USE_STEP && ML_USE_STEP <= 32 - BIAS, "order of the side jobs");
  VSTEPS(0, ML_PUB_STEP)
  WG_BARRIER();
  if (w == 7 && g == 0 && bpp == 0) {
    const f32x4 m0 = *(const f32x4*)(smem + L_MX + headn * 32), m1 = *(const f32x4*)(smem + L_MX + headn * 32 + 16);
    const f32x4 l0 = *(const f32x4*)(smem + L_LS + headn * 32), l1 = *(const f32x4*)(smem + L_LS + headn * 32 + 16);
    const float M = fmaxf(fmaxf(fmaxf(m0[0], m0[1]), fmaxf(m0[2], m0[3])), fmaxf(fmaxf(m1[0], m1[1]), fmaxf(m1[2], m1[3])));
    float L = 0.f;
#pragma unroll
    for (int k = 0; k < 4; ++k) L += __builtin_amdgcn_exp2f((m0[k] - M) * LOG2E) * l0[k] + __builtin_amdgcn_exp2f((m1[k] - M) * LOG2E) * l1[k];
    STSC(gran2(M, L, tag), rs_ws, WS_ML + (unsigned)((pair * 32 + sp) * 8 + headn) * 16u);
  }
  SB();
  VSTEPS(ML_PUB_STEP, H_USE_STEP)
  if (w == 2) {
    bool ok = !has_ffn | (g4_ok(gh[0], tag) & g4_ok(gh[1], tag));
    if (!__all(ok)) {
      unsigned spins = 0;
      do {
        __builtin_amdgcn_s_sleep(2); MEMFENCE();
        gh[0] = LDSC(rs_ws, ho); gh[1] = LDSC(rs_ws, ho + 2 * 896 * 16);
        VM0();
        ok = !has_ffn | (g4_ok(gh[0], tag) & g4_ok(gh[1], tag));
      } while (!__all(ok) && ++spins < SPIN_CAP);
    }
    f32x4 s = g4_val(gh[0]) + g4_val(gh[1]);
#pragma unroll
    for (int k = 0; k < 4; ++k) s[k] += dpp<0xB1>(s[k]);
    const float h0 = rbf((lane & 1) ? s[2] : s[0]), h1 = rbf((lane & 1) ? s[3] : s[1]);
    const float s0 = h0 / (1.f + __expf(-h0)), s1 = h1 / (1.f + __expf(-h1));
    v2u o; o.x = cvtpk(s0, s0); o.y = cvtpk(s1, s1);
    if (!has_ffn) { o.x = 0u; o.y = 0u; }
    *(v2u*)(smem + L_HD + ((lane >> 5) * 64 + 2 * (lane & 31)) * 4) = o;
  }
  SB();
  VSTEPS(H_USE_STEP, ML_GET_STEP)
#pragma unroll
  for (int k = 0; k < 4; ++k) gml[k] = LDSC(rs_ws, w == 5 ? mlo + k * 32 : OOB);
  SB();
  VSTEPS(ML_GET_STEP, ML_USE_STEP)
  if (w == 5) {
    bool ok = tags_ok(gml[0], tag) & tags_ok(gml[1], tag) & tags_ok(gml[2], tag) & tags_ok(gml[3], tag);
    if (!__all(ok)) {
      unsigned spins = 0;
      do {
        __builtin_amdgcn_s_sleep(2); MEMFENCE();
#pragma unroll
        for (int k = 0; k < 4; ++k) gml[k] = LDSC(rs_ws, mlo + k * 32);
        VM0();
        ok = tags_ok(gml[0], tag) & tags_ok(gml[1], tag) & tags_ok(gml[2], tag) & tags_ok(gml[3], tag);
      } while (!__all(ok) && ++spins < SPIN_CAP);
    }
#pragma unroll
    for (int k = 0; k < 4; ++k) {
      const float m = __uint_as_float(gml[k].x), l = __uint_as_float(gml[k].z);
      const float M = x16_max(row_max(m));
      const float L = x16_sum(row_sum(__builtin_amdgcn_exp2f((m - M) * LOG2E) * l));
      const int e = (2 * k + (lane >> 5)) * 8 + (lane & 7);
      if ((lane & 31) < 8) ((float*)(smem + L_FS))[e] = __builtin_amdgcn_exp2f((((const float*)(smem + L_MX))[e] - M) * LOG2E) / L;
    }
  }
  SB();
  VSTEPS(ML_USE_STEP, NKV)
  STAMP(7);
#pragma unroll
  for (int t = 0; t < 4; ++t) *(f32x4*)(smem + L_MRG + ((w * 4 + t) * 64 + lane) * 16) = acc[t];
  WG_BARRIER();
  STAMP(8);
  if (tid < 256) {
    const int hl = tid >> 5, dq = tid & 31;
    const unsigned char* src = smem + L_MRG + ((2 * (hl >> 2) + (dq & 1)) * 64 + (hl & 3) * 16 + (dq >> 1)) * 16;
    const f32x4 c0 = *(const f32x4*)(smem + L_FS + hl * 32), c1 = *(const f32x4*)(smem + L_FS + hl * 32 + 16);
    f32x4 s = {0.f, 0.f, 0.f, 0.f};
#pragma unroll
    for (int ww = 0; ww < 8; ++ww) s += (ww < 4 ? c0[ww & 3] : c1[ww & 3]) * *(const f32x4*)(src + ww * 4096);
    STSC(gran4(s, tag), rs_ws, WS_OP + (unsigned)j * OP_STR + (unsigned)tid * 16u);
  }
  SB();
  STAMP(9);
  const unsigned tpub = (unsigned)__builtin_amdgcn_s_memrealtime();
#pragma unroll
  for (int k = W_IN_LOOP; k < 8; ++k) tw[k] = WLD(k);
  SB();
  STAMP(10);
  const unsigned opo = WS_OP + (unsigned)(((w >> 2) * 128 + (ks >> 4) * 32 + 8 * (w & 3) + g) * OP_STR) + (unsigned)(((ks >> 1) & 7) * 512 + (ks & 1) * 256 + n16 * 16);
  v4u go[2];
  WAIT_TICK(tpub, OG_DELAY);
  STAMP(12);
  go[0] = LDSC(rs_ws, opo); go[1] = LDSC(rs_ws, opo + 4 * OP_STR);
  SB();
  f32x4 facc[2];
  {
    const unsigned* hd = (const unsigned*)(smem + L_HD) + ((lane >> 3) & 1) * 64 + 8 * w + g;
    const unsigned h0 = hd[0], h1 = hd[4];
    const v4u A0 = {h0 & mk[0], h0 & mk[1], h0 & mk[2], h0 & mk[3]}, A1 = {h1 & mk[0], h1 & mk[1], h1 & mk[2], h1 & mk[3]};
    facc[0] = mfma(A1, tw[2], mfma(A0, tw[0], f32x4{0.f, 0.f, 0.f, 0.f}));
    facc[1] = mfma(A1, tw[3], mfma(A0, tw[1], f32x4{0.f, 0.f, 0.f, 0.f}));
  }
  asm volatile("" :: "v"(facc[0]), "v"(facc[1]));
  SB();
  STAMP(11);
  {
    POLL2(go, opo, 4 * OP_STR);
    STAMP(13);
    f32x4 o = g4_val(go[0]) + g4_val(go[1]);
#pragma unroll
    for (int k = 0; k < 4; ++k) o[k] = x32_sum(x16_sum(o[k]));
    if (lane < 16) *(f32x4*)(smem + L_RQ + w * 256 + lane * 16) = o;
    WG_BARRIER();
    const float* os = (const float*)(smem + L_RQ) + ((lane >> 3) & 1) * 256 + 8 * w + g;
    const float v0 = (os[0] + os[64]) + (os[128] + os[192]), v1 = (os[4] + os[68]) + (os[132] + os[196]);
    const unsigned od0 = cvtpk(v0, v0), od1 = cvtpk(v1, v1);
    const v4u A0 = {od0 & mk[0], od0 & mk[1], od0 & mk[2], od0 & mk[3]}, A1 = {od1 & mk[0], od1 & mk[1], od1 & mk[2], od1 & mk[3]};
    const f32x4 r0 = mfma(A1, tw[6], mfma(A0, tw[4], facc[0]));
    const f32x4 r1 = mfma(A1, tw[7], mfma(A0, tw[5], facc[1]));
    *(f32x4*)(smem + L_FIN + ((w * 2 + 0) * 64 + lane) * 16) = r0;
    *(f32x4*)(smem + L_FIN + ((w * 2 + 1) * 64 + lane) * 16) = r1;
  }
  WG_BARRIER();
  if (tid < 128) {
    const int b = tid >> 6, cq = tid & 63;
    const unsigned char* src = smem + L_FIN + ((cq >> 5) * 64 + (2 * b + (cq & 1)) * 16 + ((cq & 31) >> 1)) * 16;
    f32x4 s = {0.f, 0.f, 0.f, 0.f};
#pragma unroll
    for (int ww = 0; ww < 8; ++ww) s += *(const f32x4*)(src + ww * 2048);
    STSC(gran4(s, tag), rs_ws, WS_FP + (unsigned)j * FP_STR + (unsigned)tid * 16u);
  }
  STAMP(14);
  if (w == 7) {
    const unsigned fo = WS_FP + (unsigned)((((j >> 5) & 3) * 64 + (lane >> 1)) * FP_STR) + (unsigned)((j >> 7) * 1024 + (j & 31) * 32 + (lane & 1) * 16);
    { const unsigned tf = (unsigned)__builtin_amdgcn_s_memrealtime(); WAIT_TICK(tf, FG_DELAY); }
    v4u gf[2];
    gf[0] = LDSC(rs_ws, fo); gf[1] = LDSC(rs_ws, fo + 32 * FP_STR);
    POLL2(gf, fo, 32 * FP_STR);
    f32x4 s = g4_val(gf[0]) + g4_val(gf[1]);
#pragma unroll
    for (int k = 0; k < 4; ++k) s[k] = par_sum(s[k]);
    if (lane < 2) {
      v2u o;
      o.x = cvtpk(bf2f(xres.x) + s[0], bfhi(xres.x) + s[1]);
      o.y = cvtpk(bf2f(xres.y) + s[2], bfhi(xres.y) + s[3]);
      ((v2u*)out)[(j >> 7) * 256 + (j & 127) * 2 + lane] = o;
    }
    if (j == 0 && lane == 0) __builtin_amdgcn_raw_buffer_store_b32(epoch, rs_ws, WS_E, 0, AUX_SC1);
    STAMP(15);
  }
  STAMP_FLUSH();
}

extern "C" void kernel_launch(void* const* d_in, const int* in_sizes, int n_in, void* d_out, int out_size, void* d_ws, size_t ws_size,
                              hipStream_t stream) {
  fused_decode<<<NWG, NTHR, 0, stream>>>((const u16*)d_in[0], (const u16*)d_in[1], (const u16*)d_in[2], (const u16*)d_in[3],
                                         (const u16*)d_in[4], (const u16*)d_in[6], (const u16*)d_in[7], (u16*)d_out,
                                         (unsigned char*)d_ws);
}
```

```cpp
#include <hip/hip_runtime.h>
#include <stdint.h>

typedef unsigned v4u __attribute__((ext_vector_type(4)));
typedef unsigned v2u __attribute__((ext_vector_type(2)));
typedef float f32x4 __attribute__((ext_vector_type(4)));
typedef float f32x2 __attribute__((ext_vector_type(2)));
typedef short bf16x8 __attribute__((ext_vector_type(8)));
typedef short s16x4 __attribute__((ext_vector_type(4)));
typedef __bf16 bf16x2 __attribute__((ext_vector_type(2)));
typedef unsigned short u16;

#define PAT_C 8
#define PAT_STG 8
#define PAT_ROT 0
#define RING_W 20
#define RING_D 8
#define EARLY_N 8
#define QG_T 500
#define W_IN_LOOP 3
#define OG_DELAY 120
#define FG_DELAY 70
#define POLL_HALF 10
#define POLL_ROUNDS 4
#define ML_PUB_STEP 4
#define H_USE_STEP 8
#define ML_GET_STEP 14
#define ML_USE_STEP 26
#define EARLY_ARGS 1
#define TAU16 0
#define PACE_P0 840
#define QG_MID 4
#define PMAP 0
#define PAD_B 256

#define BIAS 0
constexpr int NWG = 256, NTHR = 512, NKV = 32 + BIAS;
constexpr int W = RING_W, RD = RING_D;
constexpr int TOPUP = (W - RD) < 8 ? (W - RD) : 8;
constexpr unsigned OP_STR = 4096 + PAD_B, FP_STR = 2048 + PAD_B;
constexpr unsigned WS_E = 0;
constexpr unsigned WS_QP = 4096;
constexpr unsigned WS_HP = WS_QP + 4 * 2 * 1024 * 16;
constexpr unsigned WS_ML = WS_HP + 4 * 2 * 896 * 16;
constexpr unsigned WS_OP = WS_ML + 8 * 32 * 8 * 16;
constexpr unsigned WS_FP = WS_OP + 256 * OP_STR;
constexpr unsigned WS_END = WS_FP + 256 * FP_STR;
constexpr unsigned WS_STAMP = WS_END;
constexpr int L_XD = 0, L_RQ = 8192, L_RH = 12288, L_QS = 16384, L_HD = 18432, L_MX = 18944, L_LS = 19200, L_FS = 19456, L_MISC = 19712,
              L_MRG = 19776, L_FIN = L_MRG + 32768, L_END = L_FIN + 32768;
constexpr unsigned SPIN_CAP = 1u << 16;
constexpr int AUX_NT = 2, AUX_SC1 = 16;
constexpr unsigned OOB = 0xFFFFFF00u;
constexpr float SCALE = 0.08838834764831845f, LOG2E = 1.4426950408889634f;

__device__ __forceinline__ float bf2f(unsigned b) { return __builtin_bit_cast(float, b << 16); }
__device__ __forceinline__ float bfhi(unsigned b) { return __builtin_bit_cast(float, b & 0xffff0000u); }
__device__ __forceinline__ unsigned cvtpk(float lo, float hi) {
  return __builtin_bit_cast(unsigned, __builtin_convertvector(f32x2{lo, hi}, bf16x2));
}
__device__ __forceinline__ float rbf(float x) { return bfhi(cvtpk(x, x)); }
__device__ __forceinline__ f32x4 mfma(v4u a, v4u b, f32x4 c) {
  return __builtin_amdgcn_mfma_f32_16x16x32_bf16(__builtin_bit_cast(bf16x8, a), __builtin_bit_cast(bf16x8, b), c, 0, 0, 0);
}
__device__ __forceinline__ f32x4 mfma16(unsigned a0, unsigned a1, unsigned b0, unsigned b1, f32x4 c) {
  return __builtin_amdgcn_mfma_f32_16x16x16bf16_1k(__builtin_bit_cast(s16x4, v2u{a0, a1}), __builtin_bit_cast(s16x4, v2u{b0, b1}), c, 0, 0, 0);
}
template <int CTRL> __device__ __forceinline__ float dpp(float x) {
  return __builtin_bit_cast(float, __builtin_amdgcn_mov_dpp(__builtin_bit_cast(int, x), CTRL, 0xf, 0xf, true));
}
template <int CTRL> __device__ __forceinline__ unsigned dppu(unsigned x) { return (unsigned)__builtin_amdgcn_mov_dpp((int)x, CTRL, 0xf, 0xf, true); }
__device__ __forceinline__ float x16_max(float x) {
  auto s = __builtin_amdgcn_permlane16_swap(__float_as_uint(x), __float_as_uint(x), false, false);
  return fmaxf(__uint_as_float(s[0]), __uint_as_float(s[1]));
}
__device__ __forceinline__ float x16_sum(float x) {
  auto s = __builtin_amdgcn_permlane16_swap(__float_as_uint(x), __float_as_uint(x), false, false);
  return __uint_as_float(s[0]) + __uint_as_float(s[1]);
}
__device__ __forceinline__ float x32_max(float x) {
  auto s = __builtin_amdgcn_permlane32_swap(__float_as_uint(x), __float_as_uint(x), false, false);
  return fmaxf(__uint_as_float(s[0]), __uint_as_float(s[1]));
}
__device__ __forceinline__ float x32_sum(float x) {
  auto s = __builtin_amdgcn_permlane32_swap(__float_as_uint(x), __float_as_uint(x), false, false);
  return __uint_as_float(s[0]) + __uint_as_float(s[1]);
}
__device__ __forceinline__ float row_sum(float x) {
  x += dpp<0xB1>(x); x += dpp<0x4E>(x); x += dpp<0x141>(x); x += dpp<0x140>(x); return x;
}
__device__ __forceinline__ float row_max(float x) {
  x = fmaxf(x, dpp<0xB1>(x)); x = fmaxf(x, dpp<0x4E>(x)); x = fmaxf(x, dpp<0x141>(x)); x = fmaxf(x, dpp<0x140>(x)); return x;
}
__device__ __forceinline__ float par_sum(float x) {
  x += dpp<0x4E>(x); x += dpp<0x124>(x); x += dpp<0x128>(x); return x32_sum(x16_sum(x));
}
#define LAS __attribute__((address_space(3)))
#define WG_BARRIER() do { asm volatile("s_waitcnt lgkmcnt(0)" ::: "memory"); __builtin_amdgcn_s_barrier(); asm volatile("" ::: "memory"); } while (0)
#define SB() __builtin_amdgcn_sched_barrier(0)
#define VM0() __builtin_amdgcn_s_waitcnt(0x0F70)
#define MEMFENCE() asm volatile("" :: "s"(ws) : "memory")
#define LDNT(rs, vo, so) __builtin_amdgcn_raw_buffer_load_b128(rs, vo, so, AUX_NT)
#define LDSC(rs, vo) __builtin_amdgcn_raw_buffer_load_b128(rs, vo, 0, AUX_SC1)
#define STSC(v, rs, vo) __builtin_amdgcn_raw_buffer_store_b128(v, rs, vo, 0, AUX_SC1)
#define WAIT_TICK(base, dt) do { unsigned d_; \
    asm volatile("s_branch 2f\n1: s_sleep 1\n2: s_memrealtime vcc\n\ts_waitcnt lgkmcnt(0)\n\ts_sub_u32 %0, vcc_lo, %1\n\ts_cmp_lt_u32 %0, %2\n\ts_cbranch_scc1 1b" \
                 : "=&s"(d_) : "s"(base), "n"(dt) : "vcc", "scc"); } while (0)
#define WAIT_WAVES() do { unsigned n_ = 0; while (__hip_atomic_load((LAS unsigned*)(smem + L_MISC), __ATOMIC_RELAXED, __HIP_MEMORY_SCOPE_WORKGROUP) < 8u && ++n_ < SPIN_CAP) __builtin_amdgcn_s_sleep(1); asm volatile("" ::: "memory"); } while (0)
__device__ __forceinline__ __amdgpu_buffer_rsrc_t mkrs(const void* p, unsigned bytes) {
  return __builtin_amdgcn_make_buffer_rsrc((void*)p, 0, (int)bytes, 0x00020000);
}
__device__ __forceinline__ bool tags_ok(v4u v, unsigned tag) { return (v.y == tag) & (v.w == tag); }
__device__ __forceinline__ v4u gran2(float a, float b, unsigned tag) {
  return v4u{__builtin_bit_cast(unsigned, a), tag, __builtin_bit_cast(unsigned, b), tag};
}
__device__ __forceinline__ v4u gran4(f32x4 v, unsigned tag) {
  v4u r;
#pragma unroll
  for (int k = 0; k < 4; ++k) r[k] = ((__float_as_uint(v[k]) + 0x80u) & 0xFFFFFF00u) | ((tag >> (8 * k)) & 0xFFu);
  return r;
}
__device__ __forceinline__ bool g4_ok(v4u v, unsigned tag) {
  const unsigned lo = __builtin_amdgcn_perm(v.y, v.x, 0x0c0c0400u), hi = __builtin_amdgcn_perm(v.w, v.z, 0x04000c0cu);
  return (lo | hi) == tag;
}
__device__ __forceinline__ f32x4 g4_val(v4u v) {
  return f32x4{__uint_as_float(v.x & 0xFFFFFF00u), __uint_as_float(v.y & 0xFFFFFF00u), __uint_as_float(v.z & 0xFFFFFF00u), __uint_as_float(v.w & 0xFFFFFF00u)};
}
struct Tag40 { unsigned t[4]; };
__device__ __forceinline__ v4u gran4w(f32x4 v, const Tag40& tg) {
  v4u r;
#pragma unroll
  for (int k = 0; k < 4; ++k) r[k] = ((__float_as_uint(v[k]) + 0x200u) & 0xFFFFFC00u) | tg.t[k];
  return r;
}
__device__ __forceinline__ bool g4w_ok(v4u v, const Tag40& tg) {
  return ((((v.x ^ tg.t[0]) | (v.y ^ tg.t[1])) | ((v.z ^ tg.t[2]) | (v.w ^ tg.t[3]))) & 0x3FFu) == 0u;
}
__device__ __forceinline__ f32x4 g4w_val(v4u v) {
  return f32x4{__uint_as_float(v.x & 0xFFFFFC00u), __uint_as_float(v.y & 0xFFFFFC00u), __uint_as_float(v.z & 0xFFFFFC00u), __uint_as_float(v.w & 0xFFFFFC00u)};
}
#define PQ_ISSUE(X, BASE, STR) do { X[0] = LDSC(rs_ws, done_ ? OOB : (BASE)); X[1] = LDSC(rs_ws, done_ ? OOB : (BASE) + (STR)); } while (0)
#define PQ_TAKE(G, X) do { \
    asm volatile("" : "+v"(X[0]), "+v"(X[1]) :: "memory"); \
    const bool c_ = !done_ & g4w_ok(X[0], tgw) & g4w_ok(X[1], tgw); \
    _Pragma("unroll") for (int k_ = 0; k_ < 4; ++k_) { G[0][k_] = c_ ? X[0][k_] : G[0][k_]; G[1][k_] = c_ ? X[1][k_] : G[1][k_]; } \
    done_ |= c_; } while (0)
#define POLL2(G, BASE, STR) do { \
    bool done_ = g4w_ok(G[0], tgw) & g4w_ok(G[1], tgw); \
    if (__all(done_)) break; \
    v4u X_[2], Y_[2]; \
    MEMFENCE(); PQ_ISSUE(X_, BASE, STR); \
    __builtin_amdgcn_s_sleep(POLL_HALF); \
    MEMFENCE(); PQ_ISSUE(Y_, BASE, STR); \
    bool fin_ = false; \
    _Pragma("unroll") for (int r_ = 0; r_ < POLL_ROUNDS; ++r_) { \
      PQ_TAKE(G, X_); \
      if (__all(done_)) { fin_ = true; break; } \
      MEMFENCE(); PQ_ISSUE(X_, BASE, STR); \
      PQ_TAKE(G, Y_); \
      if (__all(done_)) { fin_ = true; break; } \
      MEMFENCE(); PQ_ISSUE(Y_, BASE, STR); \
    } \
    if (fin_) break; \
    VM0(); \
    for (unsigned spins_ = 0; spins_ < SPIN_CAP; ++spins_) { \
      PQ_TAKE(G, X_); \
      if (__all(done_)) break; \
      __builtin_amdgcn_s_sleep(2); \
      MEMFENCE(); PQ_ISSUE(X_, BASE, STR); \
      VM0(); \
    } \
  } while (0)
#define STAMP(i) do { } while (0)
#define STAMP_FLUSH() do { } while (0)

__device__ __forceinline__ void gemv64(const v4u (&wv)[4], const unsigned* xd  , const unsigned (&mk)[4],
                                       float* red  , int lane) {
  const int g = lane >> 4, bsel = (lane >> 3) & 1;
  f32x4 a0 = {0.f, 0.f, 0.f, 0.f}, a1 = {0.f, 0.f, 0.f, 0.f};
#pragma unroll
  for (int u = 0; u < 4; ++u) {
    const v2u x2 = *(const v2u*)(xd + bsel * 1024 + 8 * u + 2 * g);
    a0 = mfma(v4u{x2.x & mk[0], x2.x & mk[1], x2.x & mk[2], x2.x & mk[3]}, wv[u], a0);
    a1 = mfma(v4u{x2.y & mk[0], x2.y & mk[1], x2.y & mk[2], x2.y & mk[3]}, wv[u], a1);
  }
  f32x4 r;
#pragma unroll
  for (int i = 0; i < 4; ++i) { const float v = bsel ? a1[i] : a0[i]; r[i] = v + dpp<0x128>(v); }
  if (bsel == 0) *(f32x4*)(red + (g >> 1) * 64 + 8 * (lane & 7) + 4 * (g & 1)) = r;
}

__global__ void __launch_bounds__(NTHR, 2)
fused_decode(const u16* __restrict__ x, const u16* __restrict__ kc, const u16* __restrict__ vc, const u16* __restrict__ wq,
             const u16* __restrict__ wo, const u16* __restrict__ w1, const u16* __restrict__ w2, u16* __restrict__ out,
             unsigned char* ws) {
  __shared__ __attribute__((aligned(16))) unsigned char smem[L_END];
  const int tid = threadIdx.x, lane = tid & 63, w = __builtin_amdgcn_readfirstlane(tid >> 6);
  const int j = blockIdx.x;
  const int g = lane >> 4, n16 = lane & 15;
  if (EARLY_ARGS) asm volatile("" :: "s"(x), "s"(kc), "s"(vc), "s"(wq), "s"(wo), "s"(w1), "s"(w2), "s"(out), "s"(ws));
  STAMP(0);
  const unsigned tstart = (unsigned)__builtin_amdgcn_s_memrealtime();
  const __amdgpu_buffer_rsrc_t rs_ws = mkrs(ws, WS_END);
  const int ks = j & 63, ns = j >> 6;
  const bool has_ffn = ks < 56;
  const unsigned ep = __builtin_amdgcn_raw_buffer_load_b32(rs_ws, WS_E, 0, AUX_SC1);
  const __amdgpu_buffer_rsrc_t rs_x = mkrs(x, 4096);
  v4u xr[2];
  {
    const unsigned xo = (w < 2) ? (unsigned)(w * 2048 + lane * 32) : OOB;
    xr[0] = __builtin_amdgcn_raw_buffer_load_b128(rs_x, xo, 0, 0);
    xr[1] = __builtin_amdgcn_raw_buffer_load_b128(rs_x, xo, 16, 0);
  }
  const v2u xres = __builtin_amdgcn_raw_buffer_load_b64(rs_x, (unsigned)((j >> 7) * 2048 + (j & 127) * 16 + (lane & 1) * 8), 0, 0);
  v4u tq[4], t1[4];
  {
    const __amdgpu_buffer_rsrc_t rs = mkrs(wq, 1024u * 8192u);
    const unsigned vo = (unsigned)(256 * ns + 32 * w + (lane >> 3)) * 8192u + ks * 128 + (lane & 7) * 16;
#pragma unroll
    for (int u = 0; u < 4; ++u) tq[u] = LDNT(rs, vo, u * 8 * 8192);
  }
  {
    const __amdgpu_buffer_rsrc_t rs = mkrs(w1, has_ffn ? 1024u * 7168u : 0u);
    const unsigned vo = (unsigned)(256 * ns + 32 * w + (lane >> 3)) * 7168u + ks * 128 + (lane & 7) * 16;
#pragma unroll
    for (int u = 0; u < 4; ++u) t1[u] = LDNT(rs, vo, u * 8 * 7168);
  }
  const int pair = PMAP ? (j & 7) : (j >> 5), sp = PMAP ? (j >> 3) : (j & 31), bb = pair >> 2, kvh = pair & 3;
  const bool odd = sp & 1;
  const unsigned char* kbase = (const unsigned char*)kc + (size_t)pair * (8u << 20);
  const unsigned char* vbase = (const unsigned char*)vc + (size_t)pair * (8u << 20);
  const __amdgpu_buffer_rsrc_t rs_k = mkrs(kbase, 8u << 20), rs_v = mkrs(vbase, 8u << 20);
  const __amdgpu_buffer_rsrc_t rs_k2 = mkrs(kbase, odd ? 0u : (8u << 20)), rs_v2 = mkrs(vbase, odd ? 0u : (8u << 20));
  const unsigned wbase = (unsigned)(sp * 8 + w) * PAT_C * 1024u;
  const unsigned kvo = wbase + ((lane >> 2) & 3) * 256 + (4 * g + (lane & 3)) * 16;
  const unsigned vvo = wbase + lane * 16;
  const int st = pair * PAT_STG + (sp * 8 + w) * PAT_ROT;
#define UOFF(n, st_) ((unsigned)((((n) + (st_)) & 31) / PAT_C) * (256u * PAT_C * 1024u) + (unsigned)((((n) + (st_)) & 31) % PAT_C) * 1024u)
#define KVLD(rs, rs2, vo, i) ((i) < 32 - BIAS ? LDNT(rs, vo, UOFF(i, st)) : (i) < 32 ? LDNT(rs2, vo, UOFF(i, st)) : LDNT(rs2, vo, UOFF((i) - BIAS, st + 8 * PAT_ROT) + 8u * PAT_C * 1024u))
#define ITEM(i) ((i) < NKV ? KVLD(rs_k, rs_k2, kvo, i) : KVLD(rs_v, rs_v2, vvo, (i) - NKV))
#define PACE(i) do { if (TAU16 && (((i) - W) & 3) == 0) WAIT_TICK(tstart, PACE_P0 + ((((i) - W) * TAU16) >> 4)); } while (0)
  v4u a[W];
  SB();
  unsigned mk[4], mk2[2];
  {
    const int jp = lane & 7;
#pragma unroll
    for (int k = 0; k < 4; ++k) mk[k] = ((jp >> 1) == k) ? ((jp & 1) ? 0xffff0000u : 0x0000ffffu) : 0u;
#pragma unroll
    for (int k = 0; k < 2; ++k) mk2[k] = (((jp & 3) >> 1) == k) ? ((jp & 1) ? 0xffff0000u : 0x0000ffffu) : 0u;
  }
  if (w < 2) {
    float v[16];
#pragma unroll
    for (int k = 0; k < 4; ++k) { v[2 * k] = bf2f(xr[0][k]); v[2 * k + 1] = bfhi(xr[0][k]); v[8 + 2 * k] = bf2f(xr[1][k]); v[8 + 2 * k + 1] = bfhi(xr[1][k]); }
    float s = 0.f;
#pragma unroll
    for (int i = 0; i < 16; ++i) s += v[i];
    s = x32_sum(x16_sum(row_sum(s)));
    const float mean = s * (1.f / 1024.f);
    float q = 0.f;
#pragma unroll
    for (int i = 0; i < 16; ++i) { const float d = v[i] - mean; q += d * d; }
    q = x32_sum(x16_sum(row_sum(q)));
    const float var = rbf(q * (1.f / 1024.f));
    const float mb = rbf(mean);
    const float den = rbf(sqrtf(rbf(var + 1e-5f)));
    const float rden = 1.f / den;
    unsigned* xd = (unsigned*)(smem + L_XD) + w * 1024 + lane * 16;
#pragma unroll
    for (int k = 0; k < 4; ++k) {
      v4u o;
#pragma unroll
      for (int i = 0; i < 4; ++i) { const float t = rbf(v[4 * k + i] - mb) * rden; o[i] = cvtpk(t, t); }
      *(v4u*)(xd + 4 * k) = o;
    }
  }
  if (tid == 0) *(unsigned*)(smem + L_MISC) = 0u;
  WG_BARRIER();
  STAMP(1);
  const unsigned epoch = __builtin_amdgcn_readfirstlane(ep) + 1u;
  const unsigned tag = (epoch & 0xFFFFu) * 0x10001u;
  Tag40 tgw;
  { const unsigned ta = epoch & 0xFFFFFu, tb = (epoch * 0x9E37u + 0x1234u) & 0xFFFFFu; tgw.t[0] = ta & 0x3FFu; tgw.t[1] = ta >> 10; tgw.t[2] = tb & 0x3FFu; tgw.t[3] = tb >> 10; }
  gemv64(tq, (const unsigned*)(smem + L_XD) + 256 * ns + 32 * w, mk, (float*)(smem + L_RQ) + w * 128, lane);
  WG_BARRIER();
  if (w == 0 && lane < 32) {
    const float* red = (const float*)(smem + L_RQ) + (lane >> 4) * 64 + 4 * (lane & 15);
    f32x4 s = {0.f, 0.f, 0.f, 0.f};
#pragma unroll
    for (int ww = 0; ww < 8; ++ww) s += *(const f32x4*)(red + ww * 128);
    STSC(gran4(s, tag), rs_ws, WS_QP + (unsigned)((ns * 2 + (lane >> 4)) * 1024 + ks * 16 + (lane & 15)) * 16u);
  }
  SB();
#pragma unroll
  for (int i = 0; i < 4; ++i) a[i] = ITEM(i);
  SB();
  STAMP(2);
  gemv64(t1, (const unsigned*)(smem + L_XD) + 256 * ns + 32 * w, mk, (float*)(smem + L_RH) + w * 128, lane);
  WG_BARRIER();
  if (w == 1 && lane < 32 && has_ffn) {
    const float* red = (const float*)(smem + L_RH) + (lane >> 4) * 64 + 4 * (lane & 15);
    f32x4 s = {0.f, 0.f, 0.f, 0.f};
#pragma unroll
    for (int ww = 0; ww < 8; ++ww) s += *(const f32x4*)(red + ww * 128);
    STSC(gran4(s, tag), rs_ws, WS_HP + (unsigned)((ns * 2 + (lane >> 4)) * 896 + ks * 16 + (lane & 15)) * 16u);
  }
  SB();
#pragma unroll
  for (int i = 4; i < EARLY_N; ++i) a[i] = ITEM(i);
  SB();
  STAMP(3);
  v4u gr[2];
  {
    const unsigned qo = WS_QP + (unsigned)(((tid & 1) * 4 + bb) * 1024 + kvh * 256 + (tid >> 1)) * 16u;
    v4u gq[2];
    WAIT_TICK(tstart, QG_T);
    gq[0] = LDSC(rs_ws, qo); gq[1] = LDSC(rs_ws, qo + 2 * 1024 * 16);
    SB();
#pragma unroll
    for (int i = EARLY_N; i < EARLY_N + QG_MID; ++i) a[i] = ITEM(i);
    SB();
    bool ok = g4_ok(gq[0], tag) & g4_ok(gq[1], tag);
    gr[0] = LDSC(rs_ws, ok ? OOB : qo); gr[1] = LDSC(rs_ws, ok ? OOB : qo + 2 * 1024 * 16);
    SB();
#pragma unroll
    for (int i = EARLY_N + QG_MID; i < EARLY_N + 8; ++i) a[i] = ITEM(i);
    SB();
    STAMP(6);
    if (!__all(ok)) {
      if (!ok) { gq[0] = gr[0]; gq[1] = gr[1]; }
      ok = g4_ok(gq[0], tag) & g4_ok(gq[1], tag);
      unsigned spins = 0;
      while (!__all(ok) && ++spins < SPIN_CAP) {
        __builtin_amdgcn_s_sleep(2); MEMFENCE();
        if (!ok) { gq[0] = LDSC(rs_ws, qo); gq[1] = LDSC(rs_ws, qo + 2 * 1024 * 16); }
        VM0();
        ok = g4_ok(gq[0], tag) & g4_ok(gq[1], tag);
      }
    }
    f32x4 s = g4_val(gq[0]) + g4_val(gq[1]);
#pragma unroll
    for (int k = 0; k < 4; ++k) s[k] += dpp<0xB1>(s[k]);
    ((unsigned*)(smem + L_QS))[tid] = (tid & 1) ? cvtpk(s[2], s[3]) : cvtpk(s[0], s[1]);
  }
  WG_BARRIER();
  STAMP(4);
#pragma unroll
  for (int i = EARLY_N + 8; i < W; ++i) a[i] = ITEM(i);
  SB();
  const int headn = 4 * (n16 & 1) + (n16 >> 2), bpp = (n16 >> 1) & 1;
  v4u qb[2];
#pragma unroll
  for (int t = 0; t < 2; ++t) qb[t] = *(const v4u*)(smem + L_QS + headn * 256 + (4 * g + 2 * t + bpp) * 16);
  const unsigned ho = WS_HP + (unsigned)(((lane & 1) * 4 + (lane >> 5)) * 896 + ks * 16 + ((lane >> 1) & 15)) * 16u;
  v4u gh[2];
  {
    const bool hact = (w == 2 && has_ffn);
    gh[0] = LDSC(rs_ws, hact ? ho : OOB); gh[1] = LDSC(rs_ws, hact ? ho + 2 * 896 * 16 : OOB);
  }
  SB();
  float S[NKV];
#pragma unroll
  for (int i = 0; i < NKV; ++i) {
    const v4u cur = a[i % W];
    if (i == EARLY_N + QG_MID) asm volatile("" :: "v"(gr[0]), "v"(gr[1]));
    if (i + RD >= W) { PACE(i + RD); a[(i + RD) % W] = ITEM(i + RD); }
    const f32x4 z = {0.f, 0.f, 0.f, 0.f};
    const f32x4 d0 = mfma(cur, qb[0], z), d1 = mfma(cur, qb[1], z);
    const float t1_ = d0[0] + d1[2], t2_ = d0[1] + d1[3];
    const float x_ = bpp ? t2_ : t1_;
    S[i] = x_ + dpp<0x4E>(x_);
    if (i >= 32 - BIAS && odd) S[i] = -INFINITY;
    SB();
  }
  STAMP(5);
  float mr;
  {
    float mx = S[0];
#pragma unroll
    for (int i = 1; i < NKV; ++i) mx = fmaxf(mx, S[i]);
    mx = x32_max(x16_max(mx));
    mr = rbf(rbf(mx) * SCALE);
  }
  v4u tw[8];
  const __amdgpu_buffer_rsrc_t rs_w2 = mkrs(w2, has_ffn ? 3584u * 2048u : 0u);
  const __amdgpu_buffer_rsrc_t rs_wo = mkrs(wo, 4096u * 2048u);
  const unsigned wvo = (unsigned)(64 * ks + 8 * w + g) * 2048u + ns * 512 + n16 * 16;
#define WLD(k) ((k) < 4 ? LDNT(rs_w2, wvo, (((k) >> 1) & 1) * 4 * 2048 + ((k) & 1) * 256) : LDNT(rs_wo, wvo, (((k) >> 1) & 1) * 4 * 2048 + ((k) & 1) * 256))
  float lsum = 0.f;
  const float mc = mr * LOG2E;
  unsigned P[NKV];
#pragma unroll
  for (int i = 0; i < NKV; ++i) {
    if ((i & 3) == 0 && (i >> 2) < TOPUP) { PACE(NKV + RD + (i >> 2)); a[(NKV + RD + (i >> 2)) % W] = ITEM(NKV + RD + (i >> 2)); SB(); }
    const float sr = rbf(rbf(S[i]) * SCALE);
    const float p = __builtin_amdgcn_exp2f(sr * LOG2E - mc);
    lsum += p;
    P[i] = cvtpk(p, p);
  }
  lsum = x32_sum(x16_sum(lsum));
  if (g == 0 && bpp == 0) { ((float*)(smem + L_MX))[headn * 8 + w] = mr; ((float*)(smem + L_LS))[headn * 8 + w] = lsum; }
  asm volatile("s_waitcnt lgkmcnt(0)" ::: "memory");
  if (lane == 0) __hip_atomic_fetch_add((LAS unsigned*)(smem + L_MISC), 1u, __ATOMIC_RELAXED, __HIP_MEMORY_SCOPE_WORKGROUP);
  SB();
  f32x4 acc[4];
#pragma unroll
  for (int t = 0; t < 4; ++t) acc[t] = f32x4{0.f, 0.f, 0.f, 0.f};
  v4u gml[4];
  const unsigned mlo = WS_ML + (unsigned)((pair * 32 + (lane & 31)) * 8 + (lane >> 5)) * 16u;
#define VSTEPS(lo, hi) _Pragma("unroll") for (int i = NKV + (lo); i < NKV + (hi); ++i) { \
    const v4u cur = a[i % W]; \
    if (i + RD < 2 * NKV && i + RD >= NKV + RD + TOPUP) { PACE(i + RD); a[(i + RD) % W] = ITEM(i + RD); } \
    if (i >= 2 * NKV - W_IN_LOOP) tw[i - (2 * NKV - W_IN_LOOP)] = WLD(i - (2 * NKV - W_IN_LOOP)); \
    const unsigned pp = P[i - NKV]; \
    const unsigned p0 = dppu<0x00>(pp), p1 = dppu<0x55>(pp); \
    acc[0] = mfma16(p0 & mk2[0], p0 & mk2[1], cur.x, cur.y, acc[0]); \
    acc[1] = mfma16(p0 & mk2[0], p0 & mk2[1], cur.z, cur.w, acc[1]); \
    acc[2] = mfma16(p1 & mk2[0], p1 & mk2[1], cur.x, cur.y, acc[2]); \
    acc[3] = mfma16(p1 & mk2[0], p1 & mk2[1], cur.z, cur.w, acc[3]); \
    SB(); \
  }
  static_assert(ML_PUB_STEP <= H_USE_STEP && H_USE_STEP <= ML_GET_STEP && ML_GET_STEP <= ML_USE_STEP && ML_USE_STEP <= 32 - BIAS, "order of the side jobs");
  VSTEPS(0, ML_PUB_STEP)
  WG_BARRIER();
  if (w == 7 && g == 0 && bpp == 0) {
    const f32x4 m0 = *(const f32x4*)(smem + L_MX + headn * 32), m1 = *(const f32x4*)(smem + L_MX + headn * 32 + 16);
    const f32x4 l0 = *(const f32x4*)(smem + L_LS + headn * 32), l1 = *(const f32x4*)(smem + L_LS + headn * 32 + 16);
    const float M = fmaxf(fmaxf(fmaxf(m0[0], m0[1]), fmaxf(m0[2], m0[3])), fmaxf(fmaxf(m1[0], m1[1]), fmaxf(m1[2], m1[3])));
    float L = 0.f;
#pragma unroll
    for (int k = 0; k < 4; ++k) L += __builtin_amdgcn_exp2f((m0[k] - M) * LOG2E) * l0[k] + __builtin_amdgcn_exp2f((m1[k] - M) * LOG2E) * l1[k];
    STSC(gran2(M, L, tag), rs_ws, WS_ML + (unsigned)((pair * 32 + sp) * 8 + headn) * 16u);
  }
  SB();
  VSTEPS(ML_PUB_STEP, H_USE_STEP)
  if (w == 2) {
    bool ok = !has_ffn | (g4_ok(gh[0], tag) & g4_ok(gh[1], tag));
    if (!__all(ok)) {
      unsigned spins = 0;
      do {
        __builtin_amdgcn_s_sleep(2); MEMFENCE();
        gh[0] = LDSC(rs_ws, ho); gh[1] = LDSC(rs_ws, ho + 2 * 896 * 16);
        VM0();
        ok = !has_ffn | (g4_ok(gh[0], tag) & g4_ok(gh[1], tag));
      } while (!__all(ok) && ++spins < SPIN_CAP);
    }
    f32x4 s = g4_val(gh[0]) + g4_val(gh[1]);
#pragma unroll
    for (int k = 0; k < 4; ++k) s[k] += dpp<0xB1>(s[k]);
    const float h0 = rbf((lane & 1) ? s[2] : s[0]), h1 = rbf((lane & 1) ? s[3] : s[1]);
    const float s0 = h0 / (1.f + __expf(-h0)), s1 = h1 / (1.f + __expf(-h1));
    v2u o; o.x = cvtpk(s0, s0); o.y = cvtpk(s1, s1);
    if (!has_ffn) { o.x = 0u; o.y = 0u; }
    *(v2u*)(smem + L_HD + ((lane >> 5) * 64 + 2 * (lane & 31)) * 4) = o;
  }
  SB();
  VSTEPS(H_USE_STEP, ML_GET_STEP)
#pragma unroll
  for (int k = 0; k < 4; ++k) gml[k] = LDSC(rs_ws, w == 5 ? mlo + k * 32 : OOB);
  SB();
  VSTEPS(ML_GET_STEP, ML_USE_STEP)
  if (w == 5) {
    bool ok = tags_ok(gml[0], tag) & tags_ok(gml[1], tag) & tags_ok(gml[2], tag) & tags_ok(gml[3], tag);
    if (!__all(ok)) {
      unsigned spins = 0;
      do {
        __builtin_amdgcn_s_sleep(2); MEMFENCE();
#pragma unroll
        for (int k = 0; k < 4; ++k) gml[k] = LDSC(rs_ws, mlo + k * 32);
        VM0();
        ok = tags_ok(gml[0], tag) & tags_ok(gml[1], tag) & tags_ok(gml[2], tag) & tags_ok(gml[3], tag);
      } while (!__all(ok) && ++spins < SPIN_CAP);
    }
#pragma unroll
    for (int k = 0; k < 4; ++k) {
      const float m = __uint_as_float(gml[k].x), l = __uint_as_float(gml[k].z);
      const float M = x16_max(row_max(m));
      const float L = x16_sum(row_sum(__builtin_amdgcn_exp2f((m - M) * LOG2E) * l));
      const int e = (2 * k + (lane >> 5)) * 8 + (lane & 7);
      if ((lane & 31) < 8) ((float*)(smem + L_FS))[e] = __builtin_amdgcn_exp2f((((const float*)(smem + L_MX))[e] - M) * LOG2E) / L;
    }
  }
  SB();
  VSTEPS(ML_USE_STEP, NKV)
  STAMP(7);
#pragma unroll
  for (int t = 0; t < 4; ++t) *(f32x4*)(smem + L_MRG + ((w * 4 + t) * 64 + lane) * 16) = acc[t];
  WG_BARRIER();
  STAMP(8);
  if (tid < 256) {
    const int hl = tid >> 5, dq = tid & 31;
    const unsigned char* src = smem + L_MRG + ((2 * (hl >> 2) + (dq & 1)) * 64 + (hl & 3) * 16 + (dq >> 1)) * 16;
    const f32x4 c0 = *(const f32x4*)(smem + L_FS + hl * 32), c1 = *(const f32x4*)(smem + L_FS + hl * 32 + 16);
    f32x4 s = {0.f, 0.f, 0.f, 0.f};
#pragma unroll
    for (int ww = 0; ww < 8; ++ww) s += (ww < 4 ? c0[ww & 3] : c1[ww & 3]) * *(const f32x4*)(src + ww * 4096);
    STSC(gran4w(s, tgw), rs_ws, WS_OP + (unsigned)(pair * 32 + sp) * OP_STR + (unsigned)tid * 16u);
  }
  SB();
  STAMP(9);
  const unsigned tpub = (unsigned)__builtin_amdgcn_s_memrealtime();
#pragma unroll
  for (int k = W_IN_LOOP; k < 8; ++k) tw[k] = WLD(k);
  SB();
  STAMP(10);
  const unsigned opo = WS_OP + (unsigned)(((w >> 2) * 128 + (ks >> 4) * 32 + 8 * (w & 3) + g) * OP_STR) + (unsigned)(((ks >> 1) & 7) * 512 + (ks & 1) * 256 + n16 * 16);
  v4u go[2];
  WAIT_TICK(tpub, OG_DELAY);
  STAMP(12);
  go[0] = LDSC(rs_ws, opo); go[1] = LDSC(rs_ws, opo + 4 * OP_STR);
  SB();
  f32x4 facc[2];
  {
    const unsigned* hd = (const unsigned*)(smem + L_HD) + ((lane >> 3) & 1) * 64 + 8 * w + g;
    const unsigned h0 = hd[0], h1 = hd[4];
    const v4u A0 = {h0 & mk[0], h0 & mk[1], h0 & mk[2], h0 & mk[3]}, A1 = {h1 & mk[0], h1 & mk[1], h1 & mk[2], h1 & mk[3]};
    facc[0] = mfma(A1, tw[2], mfma(A0, tw[0], f32x4{0.f, 0.f, 0.f, 0.f}));
    facc[1] = mfma(A1, tw[3], mfma(A0, tw[1], f32x4{0.f, 0.f, 0.f, 0.f}));
  }
  asm volatile("" :: "v"(facc[0]), "v"(facc[1]));
  SB();
  STAMP(11);
  {
    POLL2(go, opo, 4 * OP_STR);
    STAMP(13);
    f32x4 o = g4w_val(go[0]) + g4w_val(go[1]);
#pragma unroll
    for (int k = 0; k < 4; ++k) o[k] = x32_sum(x16_sum(o[k]));
    if (lane < 16) *(f32x4*)(smem + L_RQ + w * 256 + lane * 16) = o;
    WG_BARRIER();
    const float* os = (const float*)(smem + L_RQ) + ((lane >> 3) & 1) * 256 + 8 * w + g;
    const float v0 = (os[0] + os[64]) + (os[128] + os[192]), v1 = (os[4] + os[68]) + (os[132] + os[196]);
    const unsigned od0 = cvtpk(v0, v0), od1 = cvtpk(v1, v1);
    const v4u A0 = {od0 & mk[0], od0 & mk[1], od0 & mk[2], od0 & mk[3]}, A1 = {od1 & mk[0], od1 & mk[1], od1 & mk[2], od1 & mk[3]};
    const f32x4 r0 = mfma(A1, tw[6], mfma(A0, tw[4], facc[0]));
    const f32x4 r1 = mfma(A1, tw[7], mfma(A0, tw[5], facc[1]));
    *(f32x4*)(smem + L_FIN + ((w * 2 + 0) * 64 + lane) * 16) = r0;
    *(f32x4*)(smem + L_FIN + ((w * 2 + 1) * 64 + lane) * 16) = r1;
  }
  WG_BARRIER();
  if (tid < 128) {
    const int b = tid >> 6, cq = tid & 63;
    const unsigned char* src = smem + L_FIN + ((cq >> 5) * 64 + (2 * b + (cq & 1)) * 16 + ((cq & 31) >> 1)) * 16;
    f32x4 s = {0.f, 0.f, 0.f, 0.f};
#pragma unroll
    for (int ww = 0; ww < 8; ++ww) s += *(const f32x4*)(src + ww * 2048);
    STSC(gran4w(s, tgw), rs_ws, WS_FP + (unsigned)j * FP_STR + (unsigned)tid * 16u);
  }
  STAMP(14);
  if (w == 7) {
    const unsigned fo = WS_FP + (unsigned)((((j >> 5) & 3) * 64 + (lane >> 1)) * FP_STR) + (unsigned)((j >> 7) * 1024 + (j & 31) * 32 + (lane & 1) * 16);
    { const unsigned tf = (unsigned)__builtin_amdgcn_s_memrealtime(); WAIT_TICK(tf, FG_DELAY); }
    v4u gf[2];
    gf[0] = LDSC(rs_ws, fo); gf[1] = LDSC(rs_ws, fo + 32 * FP_STR);
    POLL2(gf, fo, 32 * FP_STR);
    f32x4 s = g4w_val(gf[0]) + g4w_val(gf[1]);
#pragma unroll
    for (int k = 0; k < 4; ++k) s[k] = par_sum(s[k]);
    if (lane < 2) {
      v2u o;
      o.x = cvtpk(bf2f(xres.x) + s[0], bfhi(xres.x) + s[1]);
      o.y = cvtpk(bf2f(xres.y) + s[2], bfhi(xres.y) + s[3]);
      ((v2u*)out)[(j >> 7) * 256 + (j & 127) * 2 + lane] = o;
    }
    if (j == 0 && lane == 0) __builtin_amdgcn_raw_buffer_store_b32(epoch, rs_ws, WS_E, 0, AUX_SC1);
    STAMP(15);
  }
  STAMP_FLUSH();
}

extern "C" void kernel_launch(void* const* d_in, const int* in_sizes, int n_in, void* d_out, int out_size, void* d_ws, size_t ws_size,
                              hipStream_t stream) {
  fused_decode<<<NWG, NTHR, 0, stream>>>((const u16*)d_in[0], (const u16*)d_in[1], (const u16*)d_in[2], (const u16*)d_in[3],
                                         (const u16*)d_in[4], (const u16*)d_in[6], (const u16*)d_in[7], (u16*)d_out,
                                         (unsigned char*)d_ws);
}
```

```cpp
#include <hip/hip_runtime.h>
#include <stdint.h>

typedef unsigned v4u __attribute__((ext_vector_type(4)));
typedef unsigned v2u __attribute__((ext_vector_type(2)));
typedef float f32x4 __attribute__((ext_vector_type(4)));
typedef float f32x2 __attribute__((ext_vector_type(2)));
typedef short bf16x8 __attribute__((ext_vector_type(8)));
typedef short s16x4 __attribute__((ext_vector_type(4)));
typedef __bf16 bf16x2 __attribute__((ext_vector_type(2)));
typedef unsigned short u16;

#define PAT_C 8
#define PAT_STG 8
#define PAT_ROT 0
#define RING_W 20
#define RING_D 8
#define EARLY_N 8
#define QG_T 540
#define W_IN_LOOP 3
#define OG_DELAY 120
#define FG_DELAY 70
#define POLL_HALF 10
#define POLL_ROUNDS 4
#define ML_PUB_STEP 4
#define H_USE_STEP 8
#define ML_GET_STEP 14
#define ML_USE_STEP 26
#define QG_MID 4
#define W_LOOP_START 29
#define TOUCH 1
#define PAD_B 256

constexpr int NWG = 256, NTHR = 512, NKV = 32;
constexpr int W = RING_W, RD = RING_D;
constexpr int TOPUP = (W - RD) < 8 ? (W - RD) : 8;
constexpr unsigned OP_STR = 4096 + PAD_B, FP_STR = 2048 + PAD_B;
constexpr unsigned WS_E = 0;
constexpr unsigned WS_QP = 4096;
constexpr unsigned WS_HP = WS_QP + 4 * 2 * 1024 * 16;
constexpr unsigned WS_ML = WS_HP + 4 * 2 * 896 * 16;
constexpr unsigned WS_OP = WS_ML + 8 * 32 * 8 * 16;
constexpr unsigned WS_FP = WS_OP + 256 * OP_STR;
constexpr unsigned WS_END = WS_FP + 256 * FP_STR;
constexpr int L_XD = 0, L_RQ = 8192, L_RH = 12288, L_QS = 16384, L_HD = 18432, L_MX = 18944, L_LS = 19200, L_FS = 19456,
              L_MRG = 19712, L_FIN = L_MRG + 32768, L_END = L_FIN + 32768;
constexpr unsigned SPIN_CAP = 1u << 16;
constexpr int AUX_NT = 2, AUX_SC1 = 16;
constexpr unsigned OOB = 0xFFFFFF00u;
constexpr float SCALE = 0.08838834764831845f, LOG2E = 1.4426950408889634f;

__device__ __forceinline__ float bf2f(unsigned b) { return __builtin_bit_cast(float, b << 16); }
__device__ __forceinline__ float bfhi(unsigned b) { return __builtin_bit_cast(float, b & 0xffff0000u); }
__device__ __forceinline__ unsigned cvtpk(float lo, float hi) {
  return __builtin_bit_cast(unsigned, __builtin_convertvector(f32x2{lo, hi}, bf16x2));
}
__device__ __forceinline__ float rbf(float x) { return bfhi(cvtpk(x, x)); }
__device__ __forceinline__ f32x4 mfma(v4u a, v4u b, f32x4 c) {
  return __builtin_amdgcn_mfma_f32_16x16x32_bf16(__builtin_bit_cast(bf16x8, a), __builtin_bit_cast(bf16x8, b), c, 0, 0, 0);
}
__device__ __forceinline__ f32x4 mfma16(unsigned a0, unsigned a1, unsigned b0, unsigned b1, f32x4 c) {
  return __builtin_amdgcn_mfma_f32_16x16x16bf16_1k(__builtin_bit_cast(s16x4, v2u{a0, a1}), __builtin_bit_cast(s16x4, v2u{b0, b1}), c, 0, 0, 0);
}
template <int CTRL> __device__ __forceinline__ float dpp(float x) {
  return __builtin_bit_cast(float, __builtin_amdgcn_mov_dpp(__builtin_bit_cast(int, x), CTRL, 0xf, 0xf, true));
}
template <int CTRL> __device__ __forceinline__ unsigned dppu(unsigned x) { return (unsigned)__builtin_amdgcn_mov_dpp((int)x, CTRL, 0xf, 0xf, true); }
__device__ __forceinline__ float x16_max(float x) {
  auto s = __builtin_amdgcn_permlane16_swap(__float_as_uint(x), __float_as_uint(x), false, false);
  return fmaxf(__uint_as_float(s[0]), __uint_as_float(s[1]));
}
__device__ __forceinline__ float x16_sum(float x) {
  auto s = __builtin_amdgcn_permlane16_swap(__float_as_uint(x), __float_as_uint(x), false, false);
  return __uint_as_float(s[0]) + __uint_as_float(s[1]);
}
__device__ __forceinline__ float x32_max(float x) {
  auto s = __builtin_amdgcn_permlane32_swap(__float_as_uint(x), __float_as_uint(x), false, false);
  return fmaxf(__uint_as_float(s[0]), __uint_as_float(s[1]));
}
__device__ __forceinline__ float x32_sum(float x) {
  auto s = __builtin_amdgcn_permlane32_swap(__float_as_uint(x), __float_as_uint(x), false, false);
  return __uint_as_float(s[0]) + __uint_as_float(s[1]);
}
__device__ __forceinline__ float row_sum(float x) {
  x += dpp<0xB1>(x); x += dpp<0x4E>(x); x += dpp<0x141>(x); x += dpp<0x140>(x); return x;
}
__device__ __forceinline__ float row_max(float x) {
  x = fmaxf(x, dpp<0xB1>(x)); x = fmaxf(x, dpp<0x4E>(x)); x = fmaxf(x, dpp<0x141>(x)); x = fmaxf(x, dpp<0x140>(x)); return x;
}
__device__ __forceinline__ float par_sum(float x) {
  x += dpp<0x4E>(x); x += dpp<0x124>(x); x += dpp<0x128>(x); return x32_sum(x16_sum(x));
}
#define WG_BARRIER() do { asm volatile("s_waitcnt lgkmcnt(0)" ::: "memory"); __builtin_amdgcn_s_barrier(); asm volatile("" ::: "memory"); } while (0)
#define SB() __builtin_amdgcn_sched_barrier(0)
#define VM0() __builtin_amdgcn_s_waitcnt(0x0F70)
#define MEMFENCE() asm volatile("" :: "s"(ws) : "memory")
#define LDNT(rs, vo, so) __builtin_amdgcn_raw_buffer_load_b128(rs, vo, so, AUX_NT)
#define LDSC(rs, vo) __builtin_amdgcn_raw_buffer_load_b128(rs, vo, 0, AUX_SC1)
#define STSC(v, rs, vo) __builtin_amdgcn_raw_buffer_store_b128(v, rs, vo, 0, AUX_SC1)
#define WAIT_TICK(base, dt) do { unsigned d_; \
    asm volatile("s_branch 2f\n1: s_sleep 1\n2: s_memrealtime vcc\n\ts_waitcnt lgkmcnt(0)\n\ts_sub_u32 %0, vcc_lo, %1\n\ts_cmp_lt_u32 %0, %2\n\ts_cbranch_scc1 1b" \
                 : "=&s"(d_) : "s"(base), "n"(dt) : "vcc", "scc"); } while (0)
__device__ __forceinline__ __amdgpu_buffer_rsrc_t mkrs(const void* p, unsigned bytes) {
  return __builtin_amdgcn_make_buffer_rsrc((void*)p, 0, (int)bytes, 0x00020000);
}
__device__ __forceinline__ bool tags_ok(v4u v, unsigned tag) { return (v.y == tag) & (v.w == tag); }
__device__ __forceinline__ v4u gran2(float a, float b, unsigned tag) {
  return v4u{__builtin_bit_cast(unsigned, a), tag, __builtin_bit_cast(unsigned, b), tag};
}
__device__ __forceinline__ v4u gran4(f32x4 v, unsigned tag) {
  v4u r;
#pragma unroll
  for (int k = 0; k < 4; ++k) r[k] = ((__float_as_uint(v[k]) + 0x80u) & 0xFFFFFF00u) | ((tag >> (8 * k)) & 0xFFu);
  return r;
}
__device__ __forceinline__ bool g4_ok(v4u v, unsigned tag) {
  const unsigned lo = __builtin_amdgcn_perm(v.y, v.x, 0x0c0c0400u), hi = __builtin_amdgcn_perm(v.w, v.z, 0x04000c0cu);
  return (lo | hi) == tag;
}
__device__ __forceinline__ f32x4 g4_val(v4u v) {
  return f32x4{__uint_as_float(v.x & 0xFFFFFF00u), __uint_as_float(v.y & 0xFFFFFF00u), __uint_as_float(v.z & 0xFFFFFF00u), __uint_as_float(v.w & 0xFFFFFF00u)};
}
struct Tag40 { unsigned t[4]; };
__device__ __forceinline__ v4u gran4w(f32x4 v, const Tag40& tg) {
  v4u r;
#pragma unroll
  for (int k = 0; k < 4; ++k) r[k] = ((__float_as_uint(v[k]) + 0x200u) & 0xFFFFFC00u) | tg.t[k];
  return r;
}
__device__ __forceinline__ bool g4w_ok(v4u v, const Tag40& tg) {
  return ((((v.x ^ tg.t[0]) | (v.y ^ tg.t[1])) | ((v.z ^ tg.t[2]) | (v.w ^ tg.t[3]))) & 0x3FFu) == 0u;
}
__device__ __forceinline__ f32x4 g4w_val(v4u v) {
  return f32x4{__uint_as_float(v.x & 0xFFFFFC00u), __uint_as_float(v.y & 0xFFFFFC00u), __uint_as_float(v.z & 0xFFFFFC00u), __uint_as_float(v.w & 0xFFFFFC00u)};
}
#define PQ_ISSUE(X, BASE, STR) do { X[0] = LDSC(rs_ws, done_ ? OOB : (BASE)); X[1] = LDSC(rs_ws, done_ ? OOB : (BASE) + (STR)); } while (0)
#define PQ_TAKE(G, X) do { \
    asm volatile("" : "+v"(X[0]), "+v"(X[1]) :: "memory"); \
    const bool c_ = !done_ & g4w_ok(X[0], tgw) & g4w_ok(X[1], tgw); \
    _Pragma("unroll") for (int k_ = 0; k_ < 4; ++k_) { G[0][k_] = c_ ? X[0][k_] : G[0][k_]; G[1][k_] = c_ ? X[1][k_] : G[1][k_]; } \
    done_ |= c_; } while (0)
#define POLL2(G, BASE, STR) do { \
    bool done_ = g4w_ok(G[0], tgw) & g4w_ok(G[1], tgw); \
    if (__all(done_)) break; \
    v4u X_[2], Y_[2]; \
    MEMFENCE(); PQ_ISSUE(X_, BASE, STR); \
    __builtin_amdgcn_s_sleep(POLL_HALF); \
    MEMFENCE(); PQ_ISSUE(Y_, BASE, STR); \
    bool fin_ = false; \
    _Pragma("unroll") for (int r_ = 0; r_ < POLL_ROUNDS; ++r_) { \
      PQ_TAKE(G, X_); \
      if (__all(done_)) { fin_ = true; break; } \
      MEMFENCE(); PQ_ISSUE(X_, BASE, STR); \
      PQ_TAKE(G, Y_); \
      if (__all(done_)) { fin_ = true; break; } \
      MEMFENCE(); PQ_ISSUE(Y_, BASE, STR); \
    } \
    if (fin_) break; \
    VM0(); \
    for (unsigned spins_ = 0; spins_ < SPIN_CAP; ++spins_) { \
      PQ_TAKE(G, X_); \
      if (__all(done_)) break; \
      __builtin_amdgcn_s_sleep(2); \
      MEMFENCE(); PQ_ISSUE(X_, BASE, STR); \
      VM0(); \
    } \
  } while (0)

__device__ __forceinline__ void gemv64(const v4u (&wv)[4], const unsigned* xd  , const unsigned (&mk)[4],
                                       float* red  , int lane) {
  const int g = lane >> 4, bsel = (lane >> 3) & 1;
  f32x4 a0 = {0.f, 0.f, 0.f, 0.f}, a1 = {0.f, 0.f, 0.f, 0.f};
#pragma unroll
  for (int u = 0; u < 4; ++u) {
    const v2u x2 = *(const v2u*)(xd + bsel * 1024 + 8 * u + 2 * g);
    a0 = mfma(v4u{x2.x & mk[0], x2.x & mk[1], x2.x & mk[2], x2.x & mk[3]}, wv[u], a0);
    a1 = mfma(v4u{x2.y & mk[0], x2.y & mk[1], x2.y & mk[2], x2.y & mk[3]}, wv[u], a1);
  }
  f32x4 r;
#pragma unroll
  for (int i = 0; i < 4; ++i) { const float v = bsel ? a1[i] : a0[i]; r[i] = v + dpp<0x128>(v); }
  if (bsel == 0) *(f32x4*)(red + (g >> 1) * 64 + 8 * (lane & 7) + 4 * (g & 1)) = r;
}

__global__ void __launch_bounds__(NTHR, 2)
fused_decode(const u16* __restrict__ x, const u16* __restrict__ kc, const u16* __restrict__ vc, const u16* __restrict__ wq,
             const u16* __restrict__ wo, const u16* __restrict__ w1, const u16* __restrict__ w2, u16* __restrict__ out,
             unsigned char* ws) {
  __shared__ __attribute__((aligned(16))) unsigned char smem[L_END];
  const int tid = threadIdx.x, lane = tid & 63, w = __builtin_amdgcn_readfirstlane(tid >> 6);
  const int j = blockIdx.x;
  const int g = lane >> 4, n16 = lane & 15;
  asm volatile("" :: "s"(x), "s"(kc), "s"(vc), "s"(wq), "s"(wo), "s"(w1), "s"(w2), "s"(out), "s"(ws));
  const unsigned tstart = (unsigned)__builtin_amdgcn_s_memrealtime();
  const __amdgpu_buffer_rsrc_t rs_ws = mkrs(ws, WS_END);
  const int ks = j & 63, ns = j >> 6;
  const bool has_ffn = ks < 56;
  const unsigned ep = __builtin_amdgcn_raw_buffer_load_b32(rs_ws, WS_E, 0, AUX_SC1);
  const __amdgpu_buffer_rsrc_t rs_x = mkrs(x, 4096);
  v4u xr[2];
  {
    const unsigned xo = (w < 2) ? (unsigned)(w * 2048 + lane * 32) : OOB;
    xr[0] = __builtin_amdgcn_raw_buffer_load_b128(rs_x, xo, 0, 0);
    xr[1] = __builtin_amdgcn_raw_buffer_load_b128(rs_x, xo, 16, 0);
  }
  const v2u xres = __builtin_amdgcn_raw_buffer_load_b64(rs_x, (unsigned)((j >> 7) * 2048 + (j & 127) * 16 + (lane & 1) * 8), 0, 0);
  v4u tq[4], t1[4];
  {
    const __amdgpu_buffer_rsrc_t rs = mkrs(wq, 1024u * 8192u);
    const unsigned vo = (unsigned)(256 * ns + 32 * w + (lane >> 3)) * 8192u + ks * 128 + (lane & 7) * 16;
#pragma unroll
    for (int u = 0; u < 4; ++u) tq[u] = LDNT(rs, vo, u * 8 * 8192);
  }
  {
    const __amdgpu_buffer_rsrc_t rs = mkrs(w1, has_ffn ? 1024u * 7168u : 0u);
    const unsigned vo = (unsigned)(256 * ns + 32 * w + (lane >> 3)) * 7168u + ks * 128 + (lane & 7) * 16;
#pragma unroll
    for (int u = 0; u < 4; ++u) t1[u] = LDNT(rs, vo, u * 8 * 7168);
  }
  const int pair = j >> 5, sp = j & 31, bb = pair >> 2, kvh = pair & 3;
  const __amdgpu_buffer_rsrc_t rs_k = mkrs((const unsigned char*)kc + (size_t)pair * (8u << 20), 8u << 20);
  const __amdgpu_buffer_rsrc_t rs_v = mkrs((const unsigned char*)vc + (size_t)pair * (8u << 20), 8u << 20);
  const unsigned wbase = (unsigned)(sp * 8 + w) * PAT_C * 1024u;
  const unsigned kvo = wbase + ((lane >> 2) & 3) * 256 + (4 * g + (lane & 3)) * 16;
  const unsigned vvo = wbase + lane * 16;
  const int st = pair * PAT_STG + (sp * 8 + w) * PAT_ROT;
#define UOFF(n) ((unsigned)((((n) + st) & 31) / PAT_C) * (256u * PAT_C * 1024u) + (unsigned)((((n) + st) & 31) % PAT_C) * 1024u)
#define ITEM(i) ((i) < NKV ? LDNT(rs_k, kvo, UOFF(i)) : LDNT(rs_v, vvo, UOFF((i) - NKV)))
  v4u a[W];
  const bool tw_ = TOUCH && w == 3;
  const unsigned touch0 = __builtin_amdgcn_raw_buffer_load_b32(rs_k, tw_ && lane < 4 ? wbase + (unsigned)lane * (2u << 20) : OOB, 0, AUX_NT);
  const unsigned touch1 = __builtin_amdgcn_raw_buffer_load_b32(rs_v, tw_ && lane < 4 ? wbase + (unsigned)lane * (2u << 20) : OOB, 0, AUX_NT);
  const unsigned touch2 = __builtin_amdgcn_raw_buffer_load_b32(mkrs(wo, 4096u * 2048u), tw_ && lane == 0 ? (unsigned)(64 * ks) * 2048u : OOB, 0, AUX_NT);
  const unsigned touch3 = __builtin_amdgcn_raw_buffer_load_b32(mkrs(w2, has_ffn ? 3584u * 2048u : 0u), tw_ && lane == 0 ? (unsigned)(64 * ks) * 2048u : OOB, 0, AUX_NT);
  SB();
  unsigned mk[4], mk2[2];
  {
    const int jp = lane & 7;
#pragma unroll
    for (int k = 0; k < 4; ++k) mk[k] = ((jp >> 1) == k) ? ((jp & 1) ? 0xffff0000u : 0x0000ffffu) : 0u;
#pragma unroll
    for (int k = 0; k < 2; ++k) mk2[k] = (((jp & 3) >> 1) == k) ? ((jp & 1) ? 0xffff0000u : 0x0000ffffu) : 0u;
  }
  if (w < 2) {
    float v[16];
#pragma unroll
    for (int k = 0; k < 4; ++k) { v[2 * k] = bf2f(xr[0][k]); v[2 * k + 1] = bfhi(xr[0][k]); v[8 + 2 * k] = bf2f(xr[1][k]); v[8 + 2 * k + 1] = bfhi(xr[1][k]); }
    float s = 0.f;
#pragma unroll
    for (int i = 0; i < 16; ++i) s += v[i];
    s = x32_sum(x16_sum(row_sum(s)));
    const float mean = s * (1.f / 1024.f);
    float q = 0.f;
#pragma unroll
    for (int i = 0; i < 16; ++i) { const float d = v[i] - mean; q += d * d; }
    q = x32_sum(x16_sum(row_sum(q)));
    const float var = rbf(q * (1.f / 1024.f));
    const float mb = rbf(mean);
    const float den = rbf(sqrtf(rbf(var + 1e-5f)));
    const float rden = 1.f / den;
    unsigned* xd = (unsigned*)(smem + L_XD) + w * 1024 + lane * 16;
#pragma unroll
    for (int k = 0; k < 4; ++k) {
      v4u o;
#pragma unroll
      for (int i = 0; i < 4; ++i) { const float t = rbf(v[4 * k + i] - mb) * rden; o[i] = cvtpk(t, t); }
      *(v4u*)(xd + 4 * k) = o;
    }
  }
  WG_BARRIER();
  const unsigned epoch = __builtin_amdgcn_readfirstlane(ep) + 1u;
  const unsigned tag = (epoch & 0xFFFFu) * 0x10001u;
  Tag40 tgw;
  { const unsigned ta = epoch & 0xFFFFFu, tb = (epoch * 0x9E37u + 0x1234u) & 0xFFFFFu; tgw.t[0] = ta & 0x3FFu; tgw.t[1] = ta >> 10; tgw.t[2] = tb & 0x3FFu; tgw.t[3] = tb >> 10; }
  gemv64(tq, (const unsigned*)(smem + L_XD) + 256 * ns + 32 * w, mk, (float*)(smem + L_RQ) + w * 128, lane);
  WG_BARRIER();
  if (w == 0 && lane < 32) {
    const float* red = (const float*)(smem + L_RQ) + (lane >> 4) * 64 + 4 * (lane & 15);
    f32x4 s = {0.f, 0.f, 0.f, 0.f};
#pragma unroll
    for (int ww = 0; ww < 8; ++ww) s += *(const f32x4*)(red + ww * 128);
    STSC(gran4(s, tag), rs_ws, WS_QP + (unsigned)((ns * 2 + (lane >> 4)) * 1024 + ks * 16 + (lane & 15)) * 16u);
  }
  SB();
#pragma unroll
  for (int i = 0; i < 4; ++i) a[i] = ITEM(i);
  SB();
  gemv64(t1, (const unsigned*)(smem + L_XD) + 256 * ns + 32 * w, mk, (float*)(smem + L_RH) + w * 128, lane);
  asm volatile("" :: "v"(touch0), "v"(touch1), "v"(touch2), "v"(touch3));
  WG_BARRIER();
  if (w == 1 && lane < 32 && has_ffn) {
    const float* red = (const float*)(smem + L_RH) + (lane >> 4) * 64 + 4 * (lane & 15);
    f32x4 s = {0.f, 0.f, 0.f, 0.f};
#pragma unroll
    for (int ww = 0; ww < 8; ++ww) s += *(const f32x4*)(red + ww * 128);
    STSC(gran4(s, tag), rs_ws, WS_HP + (unsigned)((ns * 2 + (lane >> 4)) * 896 + ks * 16 + (lane & 15)) * 16u);
  }
  SB();
#pragma unroll
  for (int i = 4; i < EARLY_N; ++i) a[i] = ITEM(i);
  SB();
  v4u gr[2];
  {
    const unsigned qo = WS_QP + (unsigned)(((tid & 1) * 4 + bb) * 1024 + kvh * 256 + (tid >> 1)) * 16u;
    v4u gq[2];
    WAIT_TICK(tstart, QG_T);
    gq[0] = LDSC(rs_ws, qo); gq[1] = LDSC(rs_ws, qo + 2 * 1024 * 16);
    SB();
#pragma unroll
    for (int i = EARLY_N; i < EARLY_N + QG_MID; ++i) a[i] = ITEM(i);
    SB();
    bool ok = g4_ok(gq[0], tag) & g4_ok(gq[1], tag);
    gr[0] = LDSC(rs_ws, ok ? OOB : qo); gr[1] = LDSC(rs_ws, ok ? OOB : qo + 2 * 1024 * 16);
    SB();
#pragma unroll
    for (int i = EARLY_N + QG_MID; i < EARLY_N + 8; ++i) a[i] = ITEM(i);
    SB();
    if (!__all(ok)) {
      if (!ok) { gq[0] = gr[0]; gq[1] = gr[1]; }
      ok = g4_ok(gq[0], tag) & g4_ok(gq[1], tag);
      unsigned spins = 0;
      while (!__all(ok) && ++spins < SPIN_CAP) {
        __builtin_amdgcn_s_sleep(2); MEMFENCE();
        if (!ok) { gq[0] = LDSC(rs_ws, qo); gq[1] = LDSC(rs_ws, qo + 2 * 1024 * 16); }
        VM0();
        ok = g4_ok(gq[0], tag) & g4_ok(gq[1], tag);
      }
    }
    f32x4 s = g4_val(gq[0]) + g4_val(gq[1]);
#pragma unroll
    for (int k = 0; k < 4; ++k) s[k] += dpp<0xB1>(s[k]);
    ((unsigned*)(smem + L_QS))[tid] = (tid & 1) ? cvtpk(s[2], s[3]) : cvtpk(s[0], s[1]);
  }
  WG_BARRIER();
#pragma unroll
  for (int i = EARLY_N + 8; i < W; ++i) a[i] = ITEM(i);
  SB();
  const int headn = 4 * (n16 & 1) + (n16 >> 2), bpp = (n16 >> 1) & 1;
  v4u qb[2];
#pragma unroll
  for (int t = 0; t < 2; ++t) qb[t] = *(const v4u*)(smem + L_QS + headn * 256 + (4 * g + 2 * t + bpp) * 16);
  const unsigned ho = WS_HP + (unsigned)(((lane & 1) * 4 + (lane >> 5)) * 896 + ks * 16 + ((lane >> 1) & 15)) * 16u;
  v4u gh[2];
  {
    const bool hact = (w == 2 && has_ffn);
    gh[0] = LDSC(rs_ws, hact ? ho : OOB); gh[1] = LDSC(rs_ws, hact ? ho + 2 * 896 * 16 : OOB);
  }
  SB();
  float S[NKV];
#pragma unroll
  for (int i = 0; i < NKV; ++i) {
    const v4u cur = a[i % W];
    if (i == EARLY_N + QG_MID) asm volatile("" :: "v"(gr[0]), "v"(gr[1]));
    if (i + RD >= W) a[(i + RD) % W] = ITEM(i + RD);
    const f32x4 z = {0.f, 0.f, 0.f, 0.f};
    const f32x4 d0 = mfma(cur, qb[0], z), d1 = mfma(cur, qb[1], z);
    const float t1_ = d0[0] + d1[2], t2_ = d0[1] + d1[3];
    const float x_ = bpp ? t2_ : t1_;
    S[i] = x_ + dpp<0x4E>(x_);
    SB();
  }
  float mr;
  {
    float mx = S[0];
#pragma unroll
    for (int i = 1; i < NKV; ++i) mx = fmaxf(mx, S[i]);
    mx = x32_max(x16_max(mx));
    mr = rbf(rbf(mx) * SCALE);
  }
  v4u tw[8];
  const __amdgpu_buffer_rsrc_t rs_w2 = mkrs(w2, has_ffn ? 3584u * 2048u : 0u);
  const __amdgpu_buffer_rsrc_t rs_wo = mkrs(wo, 4096u * 2048u);
  const unsigned wvo = (unsigned)(64 * ks + 8 * w + g) * 2048u + ns * 512 + n16 * 16;
#define WLD(k) ((k) < 4 ? LDNT(rs_w2, wvo, (((k) >> 1) & 1) * 4 * 2048 + ((k) & 1) * 256) : LDNT(rs_wo, wvo, (((k) >> 1) & 1) * 4 * 2048 + ((k) & 1) * 256))
  float lsum = 0.f;
  const float mc = mr * LOG2E;
  unsigned P[NKV];
#pragma unroll
  for (int i = 0; i < NKV; ++i) {
    if ((i & 3) == 0 && (i >> 2) < TOPUP) { a[(NKV + RD + (i >> 2)) % W] = ITEM(NKV + RD + (i >> 2)); SB(); }
    const float sr = rbf(rbf(S[i]) * SCALE);
    const float p = __builtin_amdgcn_exp2f(sr * LOG2E - mc);
    lsum += p;
    P[i] = cvtpk(p, p);
  }
  lsum = x32_sum(x16_sum(lsum));
  if (g == 0 && bpp == 0) { ((float*)(smem + L_MX))[headn * 8 + w] = mr; ((float*)(smem + L_LS))[headn * 8 + w] = lsum; }
  SB();
  f32x4 acc[4];
#pragma unroll
  for (int t = 0; t < 4; ++t) acc[t] = f32x4{0.f, 0.f, 0.f, 0.f};
  v4u gml[4];
  const unsigned mlo = WS_ML + (unsigned)((pair * 32 + (lane & 31)) * 8 + (lane >> 5)) * 16u;
#define VSTEPS(lo, hi) _Pragma("unroll") for (int i = NKV + (lo); i < NKV + (hi); ++i) { \
    const v4u cur = a[i % W]; \
    if (i + RD < 2 * NKV && i + RD >= NKV + RD + TOPUP) a[(i + RD) % W] = ITEM(i + RD); \
    if (i >= NKV + W_LOOP_START && i < NKV + W_LOOP_START + W_IN_LOOP) tw[i - (NKV + W_LOOP_START)] = WLD(i - (NKV + W_LOOP_START)); \
    const unsigned pp = P[i - NKV]; \
    const unsigned p0 = dppu<0x00>(pp), p1 = dppu<0x55>(pp); \
    acc[0] = mfma16(p0 & mk2[0], p0 & mk2[1], cur.x, cur.y, acc[0]); \
    acc[1] = mfma16(p0 & mk2[0], p0 & mk2[1], cur.z, cur.w, acc[1]); \
    acc[2] = mfma16(p1 & mk2[0], p1 & mk2[1], cur.x, cur.y, acc[2]); \
    acc[3] = mfma16(p1 & mk2[0], p1 & mk2[1], cur.z, cur.w, acc[3]); \
    SB(); \
  }
  static_assert(ML_PUB_STEP <= H_USE_STEP && H_USE_STEP <= ML_GET_STEP && ML_GET_STEP <= ML_USE_STEP && ML_USE_STEP <= 32, "order of the side jobs");
  VSTEPS(0, ML_PUB_STEP)
  WG_BARRIER();
  if (w == 7 && g == 0 && bpp == 0) {
    const f32x4 m0 = *(const f32x4*)(smem + L_MX + headn * 32), m1 = *(const f32x4*)(smem + L_MX + headn * 32 + 16);
    const f32x4 l0 = *(const f32x4*)(smem + L_LS + headn * 32), l1 = *(const f32x4*)(smem + L_LS + headn * 32 + 16);
    const float M = fmaxf(fmaxf(fmaxf(m0[0], m0[1]), fmaxf(m0[2], m0[3])), fmaxf(fmaxf(m1[0], m1[1]), fmaxf(m1[2], m1[3])));
    float L = 0.f;
#pragma unroll
    for (int k = 0; k < 4; ++k) L += __builtin_amdgcn_exp2f((m0[k] - M) * LOG2E) * l0[k] + __builtin_amdgcn_exp2f((m1[k] - M) * LOG2E) * l1[k];
    STSC(gran2(M, L, tag), rs_ws, WS_ML + (unsigned)((pair * 32 + sp) * 8 + headn) * 16u);
  }
  SB();
  VSTEPS(ML_PUB_STEP, H_USE_STEP)
  if (w == 2) {
    bool ok = !has_ffn | (g4_ok(gh[0], tag) & g4_ok(gh[1], tag));
    if (!__all(ok)) {
      unsigned spins = 0;
      do {
        __builtin_amdgcn_s_sleep(2); MEMFENCE();
        gh[0] = LDSC(rs_ws, ho); gh[1] = LDSC(rs_ws, ho + 2 * 896 * 16);
        VM0();
        ok = !has_ffn | (g4_ok(gh[0], tag) & g4_ok(gh[1], tag));
      } while (!__all(ok) && ++spins < SPIN_CAP);
    }
    f32x4 s = g4_val(gh[0]) + g4_val(gh[1]);
#pragma unroll
    for (int k = 0; k < 4; ++k) s[k] += dpp<0xB1>(s[k]);
    const float h0 = rbf((lane & 1) ? s[2] : s[0]), h1 = rbf((lane & 1) ? s[3] : s[1]);
    const float s0 = h0 / (1.f + __expf(-h0)), s1 = h1 / (1.f + __expf(-h1));
    v2u o; o.x = cvtpk(s0, s0); o.y = cvtpk(s1, s1);
    if (!has_ffn) { o.x = 0u; o.y = 0u; }
    *(v2u*)(smem + L_HD + ((lane >> 5) * 64 + 2 * (lane & 31)) * 4) = o;
  }
  SB();
  VSTEPS(H_USE_STEP, ML_GET_STEP)
#pragma unroll
  for (int k = 0; k < 4; ++k) gml[k] = LDSC(rs_ws, w == 5 ? mlo + k * 32 : OOB);
  SB();
  VSTEPS(ML_GET_STEP, ML_USE_STEP)
  if (w == 5) {
    bool ok = tags_ok(gml[0], tag) & tags_ok(gml[1], tag) & tags_ok(gml[2], tag) & tags_ok(gml[3], tag);
    if (!__all(ok)) {
      unsigned spins = 0;
      do {
        __builtin_amdgcn_s_sleep(2); MEMFENCE();
#pragma unroll
        for (int k = 0; k < 4; ++k) gml[k] = LDSC(rs_ws, mlo + k * 32);
        VM0();
        ok = tags_ok(gml[0], tag) & tags_ok(gml[1], tag) & tags_ok(gml[2], tag) & tags_ok(gml[3], tag);
      } while (!__all(ok) && ++spins < SPIN_CAP);
    }
#pragma unroll
    for (int k = 0; k < 4; ++k) {
      const float m = __uint_as_float(gml[k].x), l = __uint_as_float(gml[k].z);
      const float M = x16_max(row_max(m));
      const float L = x16_sum(row_sum(__builtin_amdgcn_exp2f((m - M) * LOG2E) * l));
      const int e = (2 * k + (lane >> 5)) * 8 + (lane & 7);
      if ((lane & 31) < 8) ((float*)(smem + L_FS))[e] = __builtin_amdgcn_exp2f((((const float*)(smem + L_MX))[e] - M) * LOG2E) / L;
    }
  }
  SB();
  VSTEPS(ML_USE_STEP, NKV)
#pragma unroll
  for (int t = 0; t < 4; ++t) *(f32x4*)(smem + L_MRG + ((w * 4 + t) * 64 + lane) * 16) = acc[t];
  WG_BARRIER();
  if (tid < 256) {
    const int hl = tid >> 5, dq = tid & 31;
    const unsigned char* src = smem + L_MRG + ((2 * (hl >> 2) + (dq & 1)) * 64 + (hl & 3) * 16 + (dq >> 1)) * 16;
    const f32x4 c0 = *(const f32x4*)(smem + L_FS + hl * 32), c1 = *(const f32x4*)(smem + L_FS + hl * 32 + 16);
    f32x4 s = {0.f, 0.f, 0.f, 0.f};
#pragma unroll
    for (int ww = 0; ww < 8; ++ww) s += (ww < 4 ? c0[ww & 3] : c1[ww & 3]) * *(const f32x4*)(src + ww * 4096);
    STSC(gran4w(s, tgw), rs_ws, WS_OP + (unsigned)j * OP_STR + (unsigned)tid * 16u);
  }
  SB();
  const unsigned tpub = (unsigned)__builtin_amdgcn_s_memrealtime();
#pragma unroll
  for (int k = W_IN_LOOP; k < 8; ++k) tw[k] = WLD(k);
  SB();
  const unsigned opo = WS_OP + (unsigned)(((w >> 2) * 128 + (ks >> 4) * 32 + 8 * (w & 3) + g) * OP_STR) + (unsigned)(((ks >> 1) & 7) * 512 + (ks & 1) * 256 + n16 * 16);
  v4u go[2];
  WAIT_TICK(tpub, OG_DELAY);
  go[0] = LDSC(rs_ws, opo); go[1] = LDSC(rs_ws, opo + 4 * OP_STR);
  SB();
  f32x4 facc[2];
  {
    const unsigned* hd = (const unsigned*)(smem + L_HD) + ((lane >> 3) & 1) * 64 + 8 * w + g;
    const unsigned h0 = hd[0], h1 = hd[4];
    const v4u A0 = {h0 & mk[0], h0 & mk[1], h0 & mk[2], h0 & mk[3]}, A1 = {h1 & mk[0], h1 & mk[1], h1 & mk[2], h1 & mk[3]};
    facc[0] = mfma(A1, tw[2], mfma(A0, tw[0], f32x4{0.f, 0.f, 0.f, 0.f}));
    facc[1] = mfma(A1, tw[3], mfma(A0, tw[1], f32x4{0.f, 0.f, 0.f, 0.f}));
  }
  asm volatile("" :: "v"(facc[0]), "v"(facc[1]));
  SB();
  {
    POLL2(go, opo, 4 * OP_STR);
    f32x4 o = g4w_val(go[0]) + g4w_val(go[1]);
#pragma unroll
    for (int k = 0; k < 4; ++k) o[k] = x32_sum(x16_sum(o[k]));
    if (lane < 16) *(f32x4*)(smem + L_RQ + w * 256 + lane * 16) = o;
    WG_BARRIER();
    const float* os = (const float*)(smem + L_RQ) + ((lane >> 3) & 1) * 256 + 8 * w + g;
    const float v0 = (os[0] + os[64]) + (os[128] + os[192]), v1 = (os[4] + os[68]) + (os[132] + os[196]);
    const unsigned od0 = cvtpk(v0, v0), od1 = cvtpk(v1, v1);
    const v4u A0 = {od0 & mk[0], od0 & mk[1], od0 & mk[2], od0 & mk[3]}, A1 = {od1 & mk[0], od1 & mk[1], od1 & mk[2], od1 & mk[3]};
    const f32x4 r0 = mfma(A1, tw[6], mfma(A0, tw[4], facc[0]));
    const f32x4 r1 = mfma(A1, tw[7], mfma(A0, tw[5], facc[1]));
    *(f32x4*)(smem + L_FIN + ((w * 2 + 0) * 64 + lane) * 16) = r0;
    *(f32x4*)(smem + L_FIN + ((w * 2 + 1) * 64 + lane) * 16) = r1;
  }
  WG_BARRIER();
  if (tid < 128) {
    const int b = tid >> 6, cq = tid & 63;
    const unsigned char* src = smem + L_FIN + ((cq >> 5) * 64 + (2 * b + (cq & 1)) * 16 + ((cq & 31) >> 1)) * 16;
    f32x4 s = {0.f, 0.f, 0.f, 0.f};
#pragma unroll
    for (int ww = 0; ww < 8; ++ww) s += *(const f32x4*)(src + ww * 2048);
    STSC(gran4w(s, tgw), rs_ws, WS_FP + (unsigned)j * FP_STR + (unsigned)tid * 16u);
  }
  if (w == 7) {
    const unsigned fo = WS_FP + (unsigned)((((j >> 5) & 3) * 64 + (lane >> 1)) * FP_STR) + (unsigned)((j >> 7) * 1024 + (j & 31) * 32 + (lane & 1) * 16);
    { const unsigned tf = (unsigned)__builtin_amdgcn_s_memrealtime(); WAIT_TICK(tf, FG_DELAY); }
    v4u gf[2];
    gf[0] = LDSC(rs_ws, fo); gf[1] = LDSC(rs_ws, fo + 32 * FP_STR);
    POLL2(gf, fo, 32 * FP_STR);
    f32x4 s = g4w_val(gf[0]) + g4w_val(gf[1]);
#pragma unroll
    for (int k = 0; k < 4; ++k) s[k] = par_sum(s[k]);
    if (lane < 2) {
      v2u o;
      o.x = cvtpk(bf2f(xres.x) + s[0], bfhi(xres.x) + s[1]);
      o.y = cvtpk(bf2f(xres.y) + s[2], bfhi(xres.y) + s[3]);
      ((v2u*)out)[(j >> 7) * 256 + (j & 127) * 2 + lane] = o;
    }
    if (j == 0 && lane == 0) __builtin_amdgcn_raw_buffer_store_b32(epoch, rs_ws, WS_E, 0, AUX_SC1);
  }
}

extern "C" void kernel_launch(void* const* d_in, const int* in_sizes, int n_in, void* d_out, int out_size, void* d_ws, size_t ws_size,
                              hipStream_t stream) {
  fused_decode<<<NWG, NTHR, 0, stream>>>((const u16*)d_in[0], (const u16*)d_in[1], (const u16*)d_in[2], (const u16*)d_in[3],
                                         (const u16*)d_in[4], (const u16*)d_in[6], (const u16*)d_in[7], (u16*)d_out,
                                         (unsigned char*)d_ws);
}
```

```cpp
#include <hip/hip_runtime.h>
#include <stdint.h>

typedef unsigned v4u __attribute__((ext_vector_type(4)));
typedef unsigned v2u __attribute__((ext_vector_type(2)));
typedef float f32x4 __attribute__((ext_vector_type(4)));
typedef float f32x2 __attribute__((ext_vector_type(2)));
typedef short bf16x8 __attribute__((ext_vector_type(8)));
typedef short s16x4 __attribute__((ext_vector_type(4)));
typedef __bf16 bf16x2 __attribute__((ext_vector_type(2)));
typedef unsigned short u16;

#define PAT_C 8
#define PAT_STG 8
#define PAT_ROT 0
#define RING_W 20
#define RING_D 8
#define EARLY_N 8
#define QG_T 540
#define W_IN_LOOP 3
#define OG_DELAY 120
#define FG_DELAY 70
#define POLL_HALF 10
#define POLL_ROUNDS 4
#define ML_PUB_STEP 4
#define H_USE_STEP 8
#define ML_GET_STEP 14
#define ML_USE_STEP 26
#define QG_MID 4
#define W_LOOP_START 29
#define PAD_B 256

constexpr int NWG = 256, NTHR = 512, NKV = 32;
constexpr int W = RING_W, RD = RING_D;
constexpr int TOPUP = (W - RD) < 8 ? (W - RD) : 8;
constexpr unsigned OP_STR = 4096 + PAD_B, FP_STR = 2048 + PAD_B;
constexpr unsigned WS_E = 0;
constexpr unsigned WS_QP = 4096;
constexpr unsigned WS_HP = WS_QP + 4 * 2 * 1024 * 16;
constexpr unsigned WS_ML = WS_HP + 4 * 2 * 896 * 16;
constexpr unsigned WS_OP = WS_ML + 8 * 32 * 8 * 16;
constexpr unsigned WS_FP = WS_OP + 256 * OP_STR;
constexpr unsigned WS_END = WS_FP + 256 * FP_STR;
constexpr int L_XD = 0, L_RQ = 8192, L_RH = 12288, L_QS = 16384, L_HD = 18432, L_MX = 18944, L_LS = 19200, L_FS = 19456,
              L_MRG = 19712, L_FIN = L_MRG + 32768, L_END = L_FIN + 32768;
constexpr unsigned SPIN_CAP = 1u << 16;
constexpr int AUX_NT = 2, AUX_SC1 = 16;
constexpr unsigned OOB = 0xFFFFFF00u;
constexpr float SCALE = 0.08838834764831845f, LOG2E = 1.4426950408889634f;

__device__ __forceinline__ float bf2f(unsigned b) { return __builtin_bit_cast(float, b << 16); }
__device__ __forceinline__ float bfhi(unsigned b) { return __builtin_bit_cast(float, b & 0xffff0000u); }
__device__ __forceinline__ unsigned cvtpk(float lo, float hi) {
  return __builtin_bit_cast(unsigned, __builtin_convertvector(f32x2{lo, hi}, bf16x2));
}
__device__ __forceinline__ float rbf(float x) { return bfhi(cvtpk(x, x)); }
__device__ __forceinline__ f32x4 mfma(v4u a, v4u b, f32x4 c) {
  return __builtin_amdgcn_mfma_f32_16x16x32_bf16(__builtin_bit_cast(bf16x8, a), __builtin_bit_cast(bf16x8, b), c, 0, 0, 0);
}
__device__ __forceinline__ f32x4 mfma16(unsigned a0, unsigned a1, unsigned b0, unsigned b1, f32x4 c) {
  return __builtin_amdgcn_mfma_f32_16x16x16bf16_1k(__builtin_bit_cast(s16x4, v2u{a0, a1}), __builtin_bit_cast(s16x4, v2u{b0, b1}), c, 0, 0, 0);
}
template <int CTRL> __device__ __forceinline__ float dpp(float x) {
  return __builtin_bit_cast(float, __builtin_amdgcn_mov_dpp(__builtin_bit_cast(int, x), CTRL, 0xf, 0xf, true));
}
template <int CTRL> __device__ __forceinline__ unsigned dppu(unsigned x) { return (unsigned)__builtin_amdgcn_mov_dpp((int)x, CTRL, 0xf, 0xf, true); }
__device__ __forceinline__ float x16_max(float x) {
  auto s = __builtin_amdgcn_permlane16_swap(__float_as_uint(x), __float_as_uint(x), false, false);
  return fmaxf(__uint_as_float(s[0]), __uint_as_float(s[1]));
}
__device__ __forceinline__ float x16_sum(float x) {
  auto s = __builtin_amdgcn_permlane16_swap(__float_as_uint(x), __float_as_uint(x), false, false);
  return __uint_as_float(s[0]) + __uint_as_float(s[1]);
}
__device__ __forceinline__ float x32_max(float x) {
  auto s = __builtin_amdgcn_permlane32_swap(__float_as_uint(x), __float_as_uint(x), false, false);
  return fmaxf(__uint_as_float(s[0]), __uint_as_float(s[1]));
}
__device__ __forceinline__ float x32_sum(float x) {
  auto s = __builtin_amdgcn_permlane32_swap(__float_as_uint(x), __float_as_uint(x), false, false);
  return __uint_as_float(s[0]) + __uint_as_float(s[1]);
}
__device__ __forceinline__ float row_sum(float x) {
  x += dpp<0xB1>(x); x += dpp<0x4E>(x); x += dpp<0x141>(x); x += dpp<0x140>(x); return x;
}
__device__ __forceinline__ float row_max(float x) {
  x = fmaxf(x, dpp<0xB1>(x)); x = fmaxf(x, dpp<0x4E>(x)); x = fmaxf(x, dpp<0x141>(x)); x = fmaxf(x, dpp<0x140>(x)); return x;
}
__device__ __forceinline__ float par_sum(float x) {
  x += dpp<0x4E>(x); x += dpp<0x124>(x); x += dpp<0x128>(x); return x32_sum(x16_sum(x));
}
#define WG_BARRIER() do { asm volatile("s_waitcnt lgkmcnt(0)" ::: "memory"); __builtin_amdgcn_s_barrier(); asm volatile("" ::: "memory"); } while (0)
#define SB() __builtin_amdgcn_sched_barrier(0)
#define VM0() __builtin_amdgcn_s_waitcnt(0x0F70)
#define MEMFENCE() asm volatile("" :: "s"(ws) : "memory")
#define LDNT(rs, vo, so) __builtin_amdgcn_raw_buffer_load_b128(rs, vo, so, AUX_NT)
#define LDSC(rs, vo) __builtin_amdgcn_raw_buffer_load_b128(rs, vo, 0, AUX_SC1)
#define STSC(v, rs, vo) __builtin_amdgcn_raw_buffer_store_b128(v, rs, vo, 0, AUX_SC1)
#define WAIT_TICK(base, dt) do { unsigned d_; \
    asm volatile("s_branch 2f\n1: s_sleep 1\n2: s_memrealtime vcc\n\ts_waitcnt lgkmcnt(0)\n\ts_sub_u32 %0, vcc_lo, %1\n\ts_cmp_lt_u32 %0, %2\n\ts_cbranch_scc1 1b" \
                 : "=&s"(d_) : "s"(base), "n"(dt) : "vcc", "scc"); } while (0)
__device__ __forceinline__ __amdgpu_buffer_rsrc_t mkrs(const void* p, unsigned bytes) {
  return __builtin_amdgcn_make_buffer_rsrc((void*)p, 0, (int)bytes, 0x00020000);
}
__device__ __forceinline__ bool tags_ok(v4u v, unsigned tag) { return (v.y == tag) & (v.w == tag); }
__device__ __forceinline__ v4u gran2(float a, float b, unsigned tag) {
  return v4u{__builtin_bit_cast(unsigned, a), tag, __builtin_bit_cast(unsigned, b), tag};
}
__device__ __forceinline__ v4u gran4(f32x4 v, unsigned tag) {
  v4u r;
#pragma unroll
  for (int k = 0; k < 4; ++k) r[k] = ((__float_as_uint(v[k]) + 0x80u) & 0xFFFFFF00u) | ((tag >> (8 * k)) & 0xFFu);
  return r;
}
__device__ __forceinline__ bool g4_ok(v4u v, unsigned tag) {
  const unsigned lo = __builtin_amdgcn_perm(v.y, v.x, 0x0c0c0400u), hi = __builtin_amdgcn_perm(v.w, v.z, 0x04000c0cu);
  return (lo | hi) == tag;
}
__device__ __forceinline__ f32x4 g4_val(v4u v) {
  return f32x4{__uint_as_float(v.x & 0xFFFFFF00u), __uint_as_float(v.y & 0xFFFFFF00u), __uint_as_float(v.z & 0xFFFFFF00u), __uint_as_float(v.w & 0xFFFFFF00u)};
}
struct Tag40 { unsigned t[4]; };
__device__ __forceinline__ v4u gran4w(f32x4 v, const Tag40& tg) {
  v4u r;
#pragma unroll
  for (int k = 0; k < 4; ++k) r[k] = ((__float_as_uint(v[k]) + 0x200u) & 0xFFFFFC00u) | tg.t[k];
  return r;
}
__device__ __forceinline__ bool g4w_ok(v4u v, const Tag40& tg) {
  return ((((v.x ^ tg.t[0]) | (v.y ^ tg.t[1])) | ((v.z ^ tg.t[2]) | (v.w ^ tg.t[3]))) & 0x3FFu) == 0u;
}
__device__ __forceinline__ f32x4 g4w_val(v4u v) {
  return f32x4{__uint_as_float(v.x & 0xFFFFFC00u), __uint_as_float(v.y & 0xFFFFFC00u), __uint_as_float(v.z & 0xFFFFFC00u), __uint_as_float(v.w & 0xFFFFFC00u)};
}
#define PQ_ISSUE(X, BASE, STR) do { X[0] = LDSC(rs_ws, done_ ? OOB : (BASE)); X[1] = LDSC(rs_ws, done_ ? OOB : (BASE) + (STR)); } while (0)
#define PQ_TAKE(G, X) do { \
    asm volatile("" : "+v"(X[0]), "+v"(X[1]) :: "memory"); \
    const bool c_ = !done_ & g4w_ok(X[0], tgw) & g4w_ok(X[1], tgw); \
    _Pragma("unroll") for (int k_ = 0; k_ < 4; ++k_) { G[0][k_] = c_ ? X[0][k_] : G[0][k_]; G[1][k_] = c_ ? X[1][k_] : G[1][k_]; } \
    done_ |= c_; } while (0)
#define POLL2(G, BASE, STR) do { \
    bool done_ = g4w_ok(G[0], tgw) & g4w_ok(G[1], tgw); \
    if (__all(done_)) break; \
    v4u X_[2], Y_[2]; \
    MEMFENCE(); PQ_ISSUE(X_, BASE, STR); \
    __builtin_amdgcn_s_sleep(POLL_HALF); \
    MEMFENCE(); PQ_ISSUE(Y_, BASE, STR); \
    bool fin_ = false; \
    _Pragma("unroll") for (int r_ = 0; r_ < POLL_ROUNDS; ++r_) { \
      PQ_TAKE(G, X_); \
      if (__all(done_)) { fin_ = true; break; } \
      MEMFENCE(); PQ_ISSUE(X_, BASE, STR); \
      PQ_TAKE(G, Y_); \
      if (__all(done_)) { fin_ = true; break; } \
      MEMFENCE(); PQ_ISSUE(Y_, BASE, STR); \
    } \
    if (fin_) break; \
    VM0(); \
    for (unsigned spins_ = 0; spins_ < SPIN_CAP; ++spins_) { \
      PQ_TAKE(G, X_); \
      if (__all(done_)) break; \
      __builtin_amdgcn_s_sleep(2); \
      MEMFENCE(); PQ_ISSUE(X_, BASE, STR); \
      VM0(); \
    } \
  } while (0)

__device__ __forceinline__ void gemv64(const v4u (&wv)[4], const unsigned* xd  , const unsigned (&mk)[4],
                                       float* red  , int lane) {
  const int g = lane >> 4, bsel = (lane >> 3) & 1;
  f32x4 a0 = {0.f, 0.f, 0.f, 0.f}, a1 = {0.f, 0.f, 0.f, 0.f};
#pragma unroll
  for (int u = 0; u < 4; ++u) {
    const v2u x2 = *(const v2u*)(xd + bsel * 1024 + 8 * u + 2 * g);
    a0 = mfma(v4u{x2.x & mk[0], x2.x & mk[1], x2.x & mk[2], x2.x & mk[3]}, wv[u], a0);
    a1 = mfma(v4u{x2.y & mk[0], x2.y & mk[1], x2.y & mk[2], x2.y & mk[3]}, wv[u], a1);
  }
  f32x4 r;
#pragma unroll
  for (int i = 0; i < 4; ++i) { const float v = bsel ? a1[i] : a0[i]; r[i] = v + dpp<0x128>(v); }
  if (bsel == 0) *(f32x4*)(red + (g >> 1) * 64 + 8 * (lane & 7) + 4 * (g & 1)) = r;
}

__global__ void __launch_bounds__(NTHR, 2)
fused_decode(const u16* __restrict__ x, const u16* __restrict__ kc, const u16* __restrict__ vc, const u16* __restrict__ wq,
             const u16* __restrict__ wo, const u16* __restrict__ w1, const u16* __restrict__ w2, u16* __restrict__ out,
             unsigned char* ws) {
  __shared__ __attribute__((aligned(16))) unsigned char smem[L_END];
  const int tid = threadIdx.x, lane = tid & 63, w = __builtin_amdgcn_readfirstlane(tid >> 6);
  const int j = blockIdx.x;
  const int g = lane >> 4, n16 = lane & 15;
  asm volatile("" :: "s"(x), "s"(kc), "s"(vc), "s"(wq), "s"(wo), "s"(w1), "s"(w2), "s"(out), "s"(ws));
  const unsigned tstart = (unsigned)__builtin_amdgcn_s_memrealtime();
  const __amdgpu_buffer_rsrc_t rs_ws = mkrs(ws, WS_END);
  const int ks = j & 63, ns = j >> 6;
  const bool has_ffn = ks < 56;
  const unsigned ep = __builtin_amdgcn_raw_buffer_load_b32(rs_ws, WS_E, 0, AUX_SC1);
  const __amdgpu_buffer_rsrc_t rs_x = mkrs(x, 4096);
  v4u xr[2];
  {
    const unsigned xo = (w < 2) ? (unsigned)(w * 2048 + lane * 32) : OOB;
    xr[0] = __builtin_amdgcn_raw_buffer_load_b128(rs_x, xo, 0, 0);
    xr[1] = __builtin_amdgcn_raw_buffer_load_b128(rs_x, xo, 16, 0);
  }
  const v2u xres = __builtin_amdgcn_raw_buffer_load_b64(rs_x, (unsigned)((j >> 7) * 2048 + (j & 127) * 16 + (lane & 1) * 8), 0, 0);
  v4u tq[4], t1[4];
  {
    const __amdgpu_buffer_rsrc_t rs = mkrs(wq, 1024u * 8192u);
    const unsigned vo = (unsigned)(256 * ns + 32 * w + (lane >> 3)) * 8192u + ks * 128 + (lane & 7) * 16;
#pragma unroll
    for (int u = 0; u < 4; ++u) tq[u] = LDNT(rs, vo, u * 8 * 8192);
  }
  {
    const __amdgpu_buffer_rsrc_t rs = mkrs(w1, has_ffn ? 1024u * 7168u : 0u);
    const unsigned vo = (unsigned)(256 * ns + 32 * w + (lane >> 3)) * 7168u + ks * 128 + (lane & 7) * 16;
#pragma unroll
    for (int u = 0; u < 4; ++u) t1[u] = LDNT(rs, vo, u * 8 * 7168);
  }
  const int pair = j >> 5, sp = j & 31, bb = pair >> 2, kvh = pair & 3;
  const __amdgpu_buffer_rsrc_t rs_k = mkrs((const unsigned char*)kc + (size_t)pair * (8u << 20), 8u << 20);
  const __amdgpu_buffer_rsrc_t rs_v = mkrs((const unsigned char*)vc + (size_t)pair * (8u << 20), 8u << 20);
  const unsigned wbase = (unsigned)(sp * 8 + w) * PAT_C * 1024u;
  const unsigned kvo = wbase + ((lane >> 2) & 3) * 256 + (4 * g + (lane & 3)) * 16;
  const unsigned vvo = wbase + lane * 16;
  const int st = pair * PAT_STG + (sp * 8 + w) * PAT_ROT;
#define UOFF(n) ((unsigned)((((n) + st) & 31) / PAT_C) * (256u * PAT_C * 1024u) + (unsigned)((((n) + st) & 31) % PAT_C) * 1024u)
#define ITEM(i) ((i) < NKV ? LDNT(rs_k, kvo, UOFF(i)) : LDNT(rs_v, vvo, UOFF((i) - NKV)))
  v4u a[W];
  SB();
  unsigned mk[4], mk2[2];
  {
    const int jp = lane & 7;
#pragma unroll
    for (int k = 0; k < 4; ++k) mk[k] = ((jp >> 1) == k) ? ((jp & 1) ? 0xffff0000u : 0x0000ffffu) : 0u;
#pragma unroll
    for (int k = 0; k < 2; ++k) mk2[k] = (((jp & 3) >> 1) == k) ? ((jp & 1) ? 0xffff0000u : 0x0000ffffu) : 0u;
  }
  if (w < 2) {
    float v[16];
#pragma unroll
    for (int k = 0; k < 4; ++k) { v[2 * k] = bf2f(xr[0][k]); v[2 * k + 1] = bfhi(xr[0][k]); v[8 + 2 * k] = bf2f(xr[1][k]); v[8 + 2 * k + 1] = bfhi(xr[1][k]); }
    float s = 0.f;
#pragma unroll
    for (int i = 0; i < 16; ++i) s += v[i];
    s = x32_sum(x16_sum(row_sum(s)));
    const float mean = s * (1.f / 1024.f);
    float q = 0.f;
#pragma unroll
    for (int i = 0; i < 16; ++i) { const float d = v[i] - mean; q += d * d; }
    q = x32_sum(x16_sum(row_sum(q)));
    const float var = rbf(q * (1.f / 1024.f));
    const float mb = rbf(mean);
    const float den = rbf(sqrtf(rbf(var + 1e-5f)));
    const float rden = 1.f / den;
    unsigned* xd = (unsigned*)(smem + L_XD) + w * 1024 + lane * 16;
#pragma unroll
    for (int k = 0; k < 4; ++k) {
      v4u o;
#pragma unroll
      for (int i = 0; i < 4; ++i) { const float t = rbf(v[4 * k + i] - mb) * rden; o[i] = cvtpk(t, t); }
      *(v4u*)(xd + 4 * k) = o;
    }
  }
  WG_BARRIER();
  const unsigned epoch = __builtin_amdgcn_readfirstlane(ep) + 1u;
  const unsigned tag = (epoch & 0xFFFFu) * 0x10001u;
  Tag40 tgw;
  { const unsigned ta = epoch & 0xFFFFFu, tb = (epoch * 0x9E37u + 0x1234u) & 0xFFFFFu; tgw.t[0] = ta & 0x3FFu; tgw.t[1] = ta >> 10; tgw.t[2] = tb & 0x3FFu; tgw.t[3] = tb >> 10; }
  gemv64(tq, (const unsigned*)(smem + L_XD) + 256 * ns + 32 * w, mk, (float*)(smem + L_RQ) + w * 128, lane);
  WG_BARRIER();
  if (w == 0 && lane < 32) {
    const float* red = (const float*)(smem + L_RQ) + (lane >> 4) * 64 + 4 * (lane & 15);
    f32x4 s = {0.f, 0.f, 0.f, 0.f};
#pragma unroll
    for (int ww = 0; ww < 8; ++ww) s += *(const f32x4*)(red + ww * 128);
    STSC(gran4(s, tag), rs_ws, WS_QP + (unsigned)((ns * 2 + (lane >> 4)) * 1024 + ks * 16 + (lane & 15)) * 16u);
  }
  SB();
#pragma unroll
  for (int i = 0; i < 4; ++i) a[i] = ITEM(i);
  SB();
  gemv64(t1, (const unsigned*)(smem + L_XD) + 256 * ns + 32 * w, mk, (float*)(smem + L_RH) + w * 128, lane);
  WG_BARRIER();
  if (w == 1 && lane < 32 && has_ffn) {
    const float* red = (const float*)(smem + L_RH) + (lane >> 4) * 64 + 4 * (lane & 15);
    f32x4 s = {0.f, 0.f, 0.f, 0.f};
#pragma unroll
    for (int ww = 0; ww < 8; ++ww) s += *(const f32x4*)(red + ww * 128);
    STSC(gran4(s, tag), rs_ws, WS_HP + (unsigned)((ns * 2 + (lane >> 4)) * 896 + ks * 16 + (lane & 15)) * 16u);
  }
  SB();
#pragma unroll
  for (int i = 4; i < EARLY_N; ++i) a[i] = ITEM(i);
  SB();
  v4u gr[2];
  {
    const unsigned qo = WS_QP + (unsigned)(((tid & 1) * 4 + bb) * 1024 + kvh * 256 + (tid >> 1)) * 16u;
    v4u gq[2];
    WAIT_TICK(tstart, QG_T);
    gq[0] = LDSC(rs_ws, qo); gq[1] = LDSC(rs_ws, qo + 2 * 1024 * 16);
    SB();
#pragma unroll
    for (int i = EARLY_N; i < EARLY_N + QG_MID; ++i) a[i] = ITEM(i);
    SB();
    bool ok = g4_ok(gq[0], tag) & g4_ok(gq[1], tag);
    gr[0] = LDSC(rs_ws, ok ? OOB : qo); gr[1] = LDSC(rs_ws, ok ? OOB : qo + 2 * 1024 * 16);
    SB();
#pragma unroll
    for (int i = EARLY_N + QG_MID; i < EARLY_N + 8; ++i) a[i] = ITEM(i);
    SB();
    if (!__all(ok)) {
      if (!ok) { gq[0] = gr[0]; gq[1] = gr[1]; }
      ok = g4_ok(gq[0], tag) & g4_ok(gq[1], tag);
      unsigned spins = 0;
      while (!__all(ok) && ++spins < SPIN_CAP) {
        __builtin_amdgcn_s_sleep(2); MEMFENCE();
        if (!ok) { gq[0] = LDSC(rs_ws, qo); gq[1] = LDSC(rs_ws, qo + 2 * 1024 * 16); }
        VM0();
        ok = g4_ok(gq[0], tag) & g4_ok(gq[1], tag);
      }
    }
    f32x4 s = g4_val(gq[0]) + g4_val(gq[1]);
#pragma unroll
    for (int k = 0; k < 4; ++k) s[k] += dpp<0xB1>(s[k]);
    ((unsigned*)(smem + L_QS))[tid] = (tid & 1) ? cvtpk(s[2], s[3]) : cvtpk(s[0], s[1]);
  }
  WG_BARRIER();
#pragma unroll
  for (int i = EARLY_N + 8; i < W; ++i) a[i] = ITEM(i);
  SB();
  const int headn = 4 * (n16 & 1) + (n16 >> 2), bpp = (n16 >> 1) & 1;
  v4u qb[2];
#pragma unroll
  for (int t = 0; t < 2; ++t) qb[t] = *(const v4u*)(smem + L_QS + headn * 256 + (4 * g + 2 * t + bpp) * 16);
  const unsigned ho = WS_HP + (unsigned)(((lane & 1) * 4 + (lane >> 5)) * 896 + ks * 16 + ((lane >> 1) & 15)) * 16u;
  v4u gh[2];
  {
    const bool hact = (w == 2 && has_ffn);
    gh[0] = LDSC(rs_ws, hact ? ho : OOB); gh[1] = LDSC(rs_ws, hact ? ho + 2 * 896 * 16 : OOB);
  }
  SB();
  float S[NKV];
#pragma unroll
  for (int i = 0; i < NKV; ++i) {
    const v4u cur = a[i % W];
    if (i == EARLY_N + QG_MID) asm volatile("" :: "v"(gr[0]), "v"(gr[1]));
    if (i + RD >= W) a[(i + RD) % W] = ITEM(i + RD);
    const f32x4 z = {0.f, 0.f, 0.f, 0.f};
    const f32x4 d0 = mfma(cur, qb[0], z), d1 = mfma(cur, qb[1], z);
    const float t1_ = d0[0] + d1[2], t2_ = d0[1] + d1[3];
    const float x_ = bpp ? t2_ : t1_;
    S[i] = x_ + dpp<0x4E>(x_);
    SB();
  }
  float mr;
  {
    float mx = S[0];
#pragma unroll
    for (int i = 1; i < NKV; ++i) mx = fmaxf(mx, S[i]);
    mx = x32_max(x16_max(mx));
    mr = rbf(rbf(mx) * SCALE);
  }
  v4u tw[8];
  const __amdgpu_buffer_rsrc_t rs_w2 = mkrs(w2, has_ffn ? 3584u * 2048u : 0u);
  const __amdgpu_buffer_rsrc_t rs_wo = mkrs(wo, 4096u * 2048u);
  const unsigned wvo = (unsigned)(64 * ks + 8 * w + g) * 2048u + ns * 512 + n16 * 16;
#define WLD(k) ((k) < 4 ? LDNT(rs_w2, wvo, (((k) >> 1) & 1) * 4 * 2048 + ((k) & 1) * 256) : LDNT(rs_wo, wvo, (((k) >> 1) & 1) * 4 * 2048 + ((k) & 1) * 256))
  float lsum = 0.f;
  const float mc = mr * LOG2E;
  unsigned P[NKV];
#pragma unroll
  for (int i = 0; i < NKV; ++i) {
    if ((i & 3) == 0 && (i >> 2) < TOPUP) { a[(NKV + RD + (i >> 2)) % W] = ITEM(NKV + RD + (i >> 2)); SB(); }
    const float sr = rbf(rbf(S[i]) * SCALE);
    const float p = __builtin_amdgcn_exp2f(sr * LOG2E - mc);
    lsum += p;
    P[i] = cvtpk(p, p);
  }
  lsum = x32_sum(x16_sum(lsum));
  if (g == 0 && bpp == 0) { ((float*)(smem + L_MX))[headn * 8 + w] = mr; ((float*)(smem + L_LS))[headn * 8 + w] = lsum; }
  SB();
  f32x4 acc[4];
#pragma unroll
  for (int t = 0; t < 4; ++t) acc[t] = f32x4{0.f, 0.f, 0.f, 0.f};
  v4u gml[4];
  const unsigned mlo = WS_ML + (unsigned)((pair * 32 + (lane & 31)) * 8 + (lane >> 5)) * 16u;
#define VSTEPS(lo, hi) _Pragma("unroll") for (int i = NKV + (lo); i < NKV + (hi); ++i) { \
    const v4u cur = a[i % W]; \
    if (i + RD < 2 * NKV && i + RD >= NKV + RD + TOPUP) a[(i + RD) % W] = ITEM(i + RD); \
    if (i >= NKV + W_LOOP_START && i < NKV + W_LOOP_START + W_IN_LOOP) tw[i - (NKV + W_LOOP_START)] = WLD(i - (NKV + W_LOOP_START)); \
    const unsigned pp = P[i - NKV]; \
    const unsigned p0 = dppu<0x00>(pp), p1 = dppu<0x55>(pp); \
    acc[0] = mfma16(p0 & mk2[0], p0 & mk2[1], cur.x, cur.y, acc[0]); \
    acc[1] = mfma16(p0 & mk2[0], p0 & mk2[1], cur.z, cur.w, acc[1]); \
    acc[2] = mfma16(p1 & mk2[0], p1 & mk2[1], cur.x, cur.y, acc[2]); \
    acc[3] = mfma16(p1 & mk2[0], p1 & mk2[1], cur.z, cur.w, acc[3]); \
    SB(); \
  }
  static_assert(ML_PUB_STEP <= H_USE_STEP && H_USE_STEP <= ML_GET_STEP && ML_GET_STEP <= ML_USE_STEP && ML_USE_STEP <= 32, "order of the side jobs");
  VSTEPS(0, ML_PUB_STEP)
  WG_BARRIER();
  if (w == 7 && g == 0 && bpp == 0) {
    const f32x4 m0 = *(const f32x4*)(smem + L_MX + headn * 32), m1 = *(const f32x4*)(smem + L_MX + headn * 32 + 16);
    const f32x4 l0 = *(const f32x4*)(smem + L_LS + headn * 32), l1 = *(const f32x4*)(smem + L_LS + headn * 32 + 16);
    const float M = fmaxf(fmaxf(fmaxf(m0[0], m0[1]), fmaxf(m0[2], m0[3])), fmaxf(fmaxf(m1[0], m1[1]), fmaxf(m1[2], m1[3])));
    float L = 0.f;
#pragma unroll
    for (int k = 0; k < 4; ++k) L += __builtin_amdgcn_exp2f((m0[k] - M) * LOG2E) * l0[k] + __builtin_amdgcn_exp2f((m1[k] - M) * LOG2E) * l1[k];
    STSC(gran2(M, L, tag), rs_ws, WS_ML + (unsigned)((pair * 32 + sp) * 8 + headn) * 16u);
  }
  SB();
  VSTEPS(ML_PUB_STEP, H_USE_STEP)
  if (w == 2) {
    bool ok = !has_ffn | (g4_ok(gh[0], tag) & g4_ok(gh[1], tag));
    if (!__all(ok)) {
      unsigned spins = 0;
      do {
        __builtin_amdgcn_s_sleep(2); MEMFENCE();
        gh[0] = LDSC(rs_ws, ho); gh[1] = LDSC(rs_ws, ho + 2 * 896 * 16);
        VM0();
        ok = !has_ffn | (g4_ok(gh[0], tag) & g4_ok(gh[1], tag));
      } while (!__all(ok) && ++spins < SPIN_CAP);
    }
    f32x4 s = g4_val(gh[0]) + g4_val(gh[1]);
#pragma unroll
    for (int k = 0; k < 4; ++k) s[k] += dpp<0xB1>(s[k]);
    const float h0 = rbf((lane & 1) ? s[2] : s[0]), h1 = rbf((lane & 1) ? s[3] : s[1]);
    const float s0 = h0 / (1.f + __expf(-h0)), s1 = h1 / (1.f + __expf(-h1));
    v2u o; o.x = cvtpk(s0, s0); o.y = cvtpk(s1, s1);
    if (!has_ffn) { o.x = 0u; o.y = 0u; }
    *(v2u*)(smem + L_HD + ((lane >> 5) * 64 + 2 * (lane & 31)) * 4) = o;
  }
  SB();
  VSTEPS(H_USE_STEP, ML_GET_STEP)
#pragma unroll
  for (int k = 0; k < 4; ++k) gml[k] = LDSC(rs_ws, w == 5 ? mlo + k * 32 : OOB);
  SB();
  VSTEPS(ML_GET_STEP, ML_USE_STEP)
  if (w == 5) {
    bool ok = tags_ok(gml[0], tag) & tags_ok(gml[1], tag) & tags_ok(gml[2], tag) & tags_ok(gml[3], tag);
    if (!__all(ok)) {
      unsigned spins = 0;
      do {
        __builtin_amdgcn_s_sleep(2); MEMFENCE();
#pragma unroll
        for (int k = 0; k < 4; ++k) gml[k] = LDSC(rs_ws, mlo + k * 32);
        VM0();
        ok = tags_ok(gml[0], tag) & tags_ok(gml[1], tag) & tags_ok(gml[2], tag) & tags_ok(gml[3], tag);
      } while (!__all(ok) && ++spins < SPIN_CAP);
    }
#pragma unroll
    for (int k = 0; k < 4; ++k) {
      const float m = __uint_as_float(gml[k].x), l = __uint_as_float(gml[k].z);
      const float M = x16_max(row_max(m));
      const float L = x16_sum(row_sum(__builtin_amdgcn_exp2f((m - M) * LOG2E) * l));
      const int e = (2 * k + (lane >> 5)) * 8 + (lane & 7);
      if ((lane & 31) < 8) ((float*)(smem + L_FS))[e] = __builtin_amdgcn_exp2f((((const float*)(smem + L_MX))[e] - M) * LOG2E) / L;
    }
  }
  SB();
  VSTEPS(ML_USE_STEP, NKV)
#pragma unroll
  for (int t = 0; t < 4; ++t) *(f32x4*)(smem + L_MRG + ((w * 4 + t) * 64 + lane) * 16) = acc[t];
  WG_BARRIER();
  if (tid < 256) {
    const int hl = tid >> 5, dq = tid & 31;
    const unsigned char* src = smem + L_MRG + ((2 * (hl >> 2) + (dq & 1)) * 64 + (hl & 3) * 16 + (dq >> 1)) * 16;
    const f32x4 c0 = *(const f32x4*)(smem + L_FS + hl * 32), c1 = *(const f32x4*)(smem + L_FS + hl * 32 + 16);
    f32x4 s = {0.f, 0.f, 0.f, 0.f};
#pragma unroll
    for (int ww = 0; ww < 8; ++ww) s += (ww < 4 ? c0[ww & 3] : c1[ww & 3]) * *(const f32x4*)(src + ww * 4096);
    STSC(gran4w(s, tgw), rs_ws, WS_OP + (unsigned)j * OP_STR + (unsigned)tid * 16u);
  }
  SB();
  const unsigned tpub = (unsigned)__builtin_amdgcn_s_memrealtime();
#pragma unroll
  for (int k = W_IN_LOOP; k < 8; ++k) tw[k] = WLD(k);
  SB();
  const unsigned opo = WS_OP + (unsigned)(((w >> 2) * 128 + (ks >> 4) * 32 + 8 * (w & 3) + g) * OP_STR) + (unsigned)(((ks >> 1) & 7) * 512 + (ks & 1) * 256 + n16 * 16);
  v4u go[2];
  WAIT_TICK(tpub, OG_DELAY);
  go[0] = LDSC(rs_ws, opo); go[1] = LDSC(rs_ws, opo + 4 * OP_STR);
  SB();
  f32x4 facc[2];
  {
    const unsigned* hd = (const unsigned*)(smem + L_HD) + ((lane >> 3) & 1) * 64 + 8 * w + g;
    const unsigned h0 = hd[0], h1 = hd[4];
    const v4u A0 = {h0 & mk[0], h0 & mk[1], h0 & mk[2], h0 & mk[3]}, A1 = {h1 & mk[0], h1 & mk[1], h1 & mk[2], h1 & mk[3]};
    facc[0] = mfma(A1, tw[2], mfma(A0, tw[0], f32x4{0.f, 0.f, 0.f, 0.f}));
    facc[1] = mfma(A1, tw[3], mfma(A0, tw[1], f32x4{0.f, 0.f, 0.f, 0.f}));
  }
  asm volatile("" :: "v"(facc[0]), "v"(facc[1]));
  SB();
  {
    POLL2(go, opo, 4 * OP_STR);
    f32x4 o = g4w_val(go[0]) + g4w_val(go[1]);
#pragma unroll
    for (int k = 0; k < 4; ++k) o[k] = x32_sum(x16_sum(o[k]));
    if (lane < 16) *(f32x4*)(smem + L_RQ + w * 256 + lane * 16) = o;
    WG_BARRIER();
    const float* os = (const float*)(smem + L_RQ) + ((lane >> 3) & 1) * 256 + 8 * w + g;
    const float v0 = (os[0] + os[64]) + (os[128] + os[192]), v1 = (os[4] + os[68]) + (os[132] + os[196]);
    const unsigned od0 = cvtpk(v0, v0), od1 = cvtpk(v1, v1);
    const v4u A0 = {od0 & mk[0], od0 & mk[1], od0 & mk[2], od0 & mk[3]}, A1 = {od1 & mk[0], od1 & mk[1], od1 & mk[2], od1 & mk[3]};
    const f32x4 r0 = mfma(A1, tw[6], mfma(A0, tw[4], facc[0]));
    const f32x4 r1 = mfma(A1, tw[7], mfma(A0, tw[5], facc[1]));
    *(f32x4*)(smem + L_FIN + ((w * 2 + 0) * 64 + lane) * 16) = r0;
    *(f32x4*)(smem + L_FIN + ((w * 2 + 1) * 64 + lane) * 16) = r1;
  }
  WG_BARRIER();
  if (tid < 128) {
    const int b = tid >> 6, cq = tid & 63;
    const unsigned char* src = smem + L_FIN + ((cq >> 5) * 64 + (2 * b + (cq & 1)) * 16 + ((cq & 31) >> 1)) * 16;
    f32x4 s = {0.f, 0.f, 0.f, 0.f};
#pragma unroll
    for (int ww = 0; ww < 8; ++ww) s += *(const f32x4*)(src + ww * 2048);
    STSC(gran4w(s, tgw), rs_ws, WS_FP + (unsigned)j * FP_STR + (unsigned)tid * 16u);
  }
  if (w == 7) {
    const unsigned fo = WS_FP + (unsigned)((((j >> 5) & 3) * 64 + (lane >> 1)) * FP_STR) + (unsigned)((j >> 7) * 1024 + (j & 31) * 32 + (lane & 1) * 16);
    { const unsigned tf = (unsigned)__builtin_amdgcn_s_memrealtime(); WAIT_TICK(tf, FG_DELAY); }
    v4u gf[2];
    gf[0] = LDSC(rs_ws, fo); gf[1] = LDSC(rs_ws, fo + 32 * FP_STR);
    POLL2(gf, fo, 32 * FP_STR);
    f32x4 s = g4w_val(gf[0]) + g4w_val(gf[1]);
#pragma unroll
    for (int k = 0; k < 4; ++k) s[k] = par_sum(s[k]);
    if (lane < 2) {
      v2u o;
      o.x = cvtpk(bf2f(xres.x) + s[0], bfhi(xres.x) + s[1]);
      o.y = cvtpk(bf2f(xres.y) + s[2], bfhi(xres.y) + s[3]);
      ((v2u*)out)[(j >> 7) * 256 + (j & 127) * 2 + lane] = o;
    }
    if (j == 0 && lane == 0) __builtin_amdgcn_raw_buffer_store_b32(epoch, rs_ws, WS_E, 0, AUX_SC1);
  }
}

extern "C" void kernel_launch(void* const* d_in, const int* in_sizes, int n_in, void* d_out, int out_size, void* d_ws, size_t ws_size,
                              hipStream_t stream) {
  fused_decode<<<NWG, NTHR, 0, stream>>>((const u16*)d_in[0], (const u16*)d_in[1], (const u16*)d_in[2], (const u16*)d_in[3],
                                         (const u16*)d_in[4], (const u16*)d_in[6], (const u16*)d_in[7], (u16*)d_out,
                                         (unsigned char*)d_ws);
}
```
